```python
import torch

def _softmax(x, axis=-1):
    return torch.softmax(x, dim=axis)
def _gelu(x):
    return torch.nn.functional.gelu(x, approximate="tanh")
def _silu(x):
    return torch.nn.functional.silu(x)
def _sigmoid(x):
    return torch.sigmoid(x)
def _layernorm(x, axis=-1, eps=1e-5):
    m = x.mean(dim=axis, keepdim=True)
    v = x.var(dim=axis, keepdim=True, unbiased=False)
    return (x - m) / torch.sqrt(v + eps)
def _scatter_add(tgt, idx, src, axis=0):
    return tgt.clone().index_add_(axis, idx, src)
def _segment_sum(src, seg, n):
    out = torch.zeros((n,) + src.shape[1:], dtype=src.dtype)
    return out.index_add_(0, seg, src)
def _pad(x, pw):
    flat = tuple(v for pair in reversed(pw) for v in pair)
    return torch.nn.functional.pad(x, flat)

kernel_name = 'gated_mix__linear_attn_block__dense_activation_mish__prompt'

def setup_inputs():

    _w = (lambda a: (lambda t: t.to(torch.bfloat16) if t.is_floating_point() else t)(torch.as_tensor(a)))
    def _A_setup():
        B, S, D = 1, 2048, 512
        return {
            "x": _w((lambda *s: torch.randn(*s, dtype=torch.bfloat16) * 0.05)(B, S, D)),
            "wqkv": _w((lambda *s: torch.randn(*s, dtype=torch.bfloat16) * 0.8)(D, 3 * D)),
            "wo": _w((lambda *s: torch.randn(*s, dtype=torch.bfloat16) * 0.1)(D, D)),
        }
    _A_in = _A_setup()

    def _B_setup():
        B, S, D, F = 1, 2048, 512, 1792
        return {
            "x": _w((lambda *s: torch.randn(*s, dtype=torch.bfloat16) * 0.05)(B, S, D)),
            "w1": _w((lambda *s: torch.randn(*s, dtype=torch.bfloat16) * 12.8)(D, F) * (D ** -0.5)),
            "b1": _w((lambda *s: torch.randn(*s, dtype=torch.bfloat16) * 0.4)(F)),
            "w2": _w((lambda *s: torch.randn(*s, dtype=torch.bfloat16) * 0.05)(F, D) * (F ** -0.5)),
        }
    _B_in = _B_setup()

    return {
        "A_x": _A_in["x"],
        "A_wqkv": _A_in["wqkv"],
        "A_wo": _A_in["wo"],
        "B_x": _B_in["x"],
        "B_w1": _B_in["w1"],
        "B_b1": _B_in["b1"],
        "B_w2": _B_in["w2"],
        "gate_w": _w((lambda *s: torch.randn(*s, dtype=torch.bfloat16) * 0.05)(512) * 16.0),
    }

def reference(A_x, A_wqkv, A_wo, B_x, B_w1, B_b1, B_w2, gate_w):
    def _A_ref(x, wqkv, wo):
        B, S, D = x.shape
        H = 32; Dh = D // H
        qkv = (lambda x, *d: x.permute(*d))((x @ wqkv).reshape(B, S, 3, H, Dh), 2, 0, 3, 1, 4)
        q, k, v = qkv[0], qkv[1], qkv[2]
        phi = lambda t: torch.where(t > 0, t + 1, torch.exp(t)) + 1e-6
        pq, pk = phi(q), phi(k)
        kv = torch.einsum("bhsd,bhse->bhde", pk, v)
        z = torch.einsum("bhsd->bhd", pk)
        num = torch.einsum("bhqd,bhde->bhqe", pq, kv)
        den = torch.einsum("bhqd,bhd->bhq", pq, z)[..., None] + 1e-6
        o = num / den
        return (lambda x, *d: x.permute(*d))(o, 0, 2, 1, 3).reshape(B, S, D) @ wo

    def _B_ref(x, w1, b1, w2):
        h = torch.einsum("bsd,df->bsf", x, w1) + b1
        sp = torch.log1p(torch.exp(-torch.abs(h))) + torch.where(h > 0, h, torch.zeros_like(h))
        act = h * torch.tanh(sp)
        return torch.einsum("bsf,fd->bsd", act, w2)

    x = A_x
    g = _sigmoid((x * gate_w).sum(-1, keepdims=True))
    a = _A_ref(x, A_wqkv, A_wo)
    b = _B_ref(x, B_w1, B_b1, B_w2)
    return g * a + (1 - g) * b
```

```cpp
#include <hip/hip_runtime.h>
#include <hip/hip_bf16.h>
typedef unsigned short u16;
__device__ __forceinline__ float bf2f(u16 b){ return __builtin_bit_cast(float, (unsigned)b<<16); }
__device__ __forceinline__ u16 f2bf(float f){ unsigned u=__builtin_bit_cast(unsigned,f); u += 0x7fffu + ((u>>16)&1u); return (u16)(u>>16); }
constexpr int S=2048,D=512,F=1792,H=32,DH=16;
__global__ void gemm(const u16* A,const u16* B,float* C,int M,int N,int K){
  int n=blockIdx.x*64+threadIdx.x%64; int m=blockIdx.y*4+threadIdx.x/64;
  float acc=0; for(int k=0;k<K;k++) acc+=bf2f(A[m*K+k])*bf2f(B[k*N+n]);
  C[m*N+n]=acc;
}
__device__ float phi(float t){ return (t>0? t+1.f : __expf(t)) + 1e-6f; }
__global__ void kvk(const float* qkv,float* kv){
  int h=blockIdx.x; int t=threadIdx.x; if(t>=16*17) return; int d=t/17,e=t%17;
  float acc=0; for(int s=0;s<S;s++){ float pk=bf2f(f2bf(phi(bf2f(f2bf(qkv[s*1536+512+h*16+d]))))); float v= e<16? bf2f(f2bf(qkv[s*1536+1024+h*16+e])):1.f; acc+=pk*v; }
  kv[(h*16+d)*17+e]=acc;
}
__global__ void ok(const float* qkv,const float* kv,u16* o){
  int i=blockIdx.x*256+threadIdx.x; int s=i/512,c=i%512,h=c/16,e=c%16;
  float num=0,den=0; for(int d=0;d<16;d++){ float pq=bf2f(f2bf(phi(bf2f(f2bf(qkv[s*1536+h*16+d]))))); num+=pq*bf2f(f2bf(kv[(h*16+d)*17+e])); den+=pq*bf2f(f2bf(kv[(h*16+d)*17+16])); }
  o[i]=f2bf(num/(den+1e-6f));
}
__global__ void actk(const float* hh,const u16* b1,u16* act){
  int i=blockIdx.x*256+threadIdx.x; float h=hh[i]+bf2f(b1[i%F]);
  float sp=log1pf(__expf(-fabsf(h)))+(h>0?h:0.f); act[i]=f2bf(h*tanhf(sp));
}
__global__ void fin(const u16* x,const u16* gw,const float* a,const float* b,u16* out){
  int i=blockIdx.x*256+threadIdx.x; int s=i/512; float acc=0; for(int k=0;k<512;k++) acc+=bf2f(x[s*512+k])*bf2f(gw[k]);
  float g=1.f/(1.f+__expf(-acc)); out[i]=f2bf(g*a[i]+(1.f-g)*b[i]);
}
extern "C" void kernel_launch(void* const* d_in, const int* in_sizes, int n_in, void* d_out, int out_size, void* d_ws, size_t ws_size, hipStream_t stream) {
  const u16* x=(const u16*)d_in[0]; const u16* wqkv=(const u16*)d_in[1]; const u16* wo=(const u16*)d_in[2];
  const u16* w1=(const u16*)d_in[4]; const u16* b1=(const u16*)d_in[5]; const u16* w2=(const u16*)d_in[6]; const u16* gw=(const u16*)d_in[7];
  char* ws=(char*)d_ws; float* qkv=(float*)ws; ws+=S*1536*4; float* hh=(float*)ws; ws+=S*F*4; float* kv=(float*)ws; ws+=65536; u16* o=(u16*)ws; ws+=S*D*2; u16* act=(u16*)ws; ws+=S*F*2; float* a=(float*)ws; ws+=S*D*4; float* b=(float*)ws;
  gemm<<<dim3(1536/64,S/4),256,0,stream>>>(x,wqkv,qkv,S,1536,D);
  gemm<<<dim3(F/64,S/4),256,0,stream>>>(x,w1,hh,S,F,D);
  kvk<<<H,320,0,stream>>>(qkv,kv);
  ok<<<S*D/256,256,0,stream>>>(qkv,kv,o);
  actk<<<S*F/256,256,0,stream>>>(hh,b1,act);
  gemm<<<dim3(D/64,S/4),256,0,stream>>>(o,wo,a,S,D,D);
  gemm<<<dim3(D/64,S/4),256,0,stream>>>(act,w2,b,S,D,F);
  fin<<<S*D/256,256,0,stream>>>(x,gw,a,b,(u16*)d_out);
}
```

```cpp
#include <hip/hip_runtime.h>
typedef unsigned short u16;
typedef unsigned u32;
typedef unsigned long long u64;
typedef short bf16x8 __attribute__((ext_vector_type(8)));
typedef short s16x4 __attribute__((ext_vector_type(4)));
typedef float f32x16 __attribute__((ext_vector_type(16)));
typedef float f32x4 __attribute__((ext_vector_type(4)));
typedef float f32x2 __attribute__((ext_vector_type(2)));
typedef __bf16 b16x2 __attribute__((ext_vector_type(2)));
typedef u32 u32x4 __attribute__((ext_vector_type(4)));
typedef u32 u32x2 __attribute__((ext_vector_type(2)));
#define LAS __attribute__((address_space(3)))
#define GAS __attribute__((address_space(1)))
#define DI __device__ __forceinline__
#define RLX_AGENT __ATOMIC_RELAXED, __HIP_MEMORY_SCOPE_AGENT
#define SB() __builtin_amdgcn_sched_barrier(0)

constexpr int SLOT = 49152, SCR = 3 * SLOT, LDS_BYTES = 163840;
constexpr int L_U = 16384, L_HALF = 40960;
constexpr int S_GATE = SCR;
constexpr int S_KVR = SCR + 1024;
constexpr int S_KVG = SCR + 1024;
constexpr int S_ZG = SCR + 9216;
constexpr int S_SCALE = SCR + 10240;
constexpr int S_MISC = SCR + 10752;
constexpr u32 W_EPOCH = 0;
constexpr u32 W_FH = 4096;
constexpr u32 W_FKV = W_FH + 16 * 8 * 8 * 128;
constexpr u32 W_FO = W_FKV + 8 * 2 * 16 * 128;
constexpr u32 W_STAMP = 512 * 1024;
constexpr u32 W_REG0 = 1u << 20;
constexpr u32 R_KV = 9u << 20;
constexpr u32 R_Z = R_KV + 512 * 1024;
constexpr u32 R_SC = R_Z + 32 * 1024;
constexpr u32 R_STRIDE = 10u << 20;
constexpr u32 NREG = 24;
constexpr u32 WS_BYTES = W_REG0 + NREG * R_STRIDE;
constexpr u32 SPIN_CAP = 1u << 20;
constexpr float LOG2E = 1.4426950408889634f;

DI u32x4 mk_srd(const void* p, u32 bytes) {
  u64 a = (u64)p;
  u32x4 r;
  r.x = (u32)a; r.y = (u32)(a >> 32) & 0xffffu; r.z = bytes; r.w = 0x00020000u;
  return r;
}
DI void dma(u32x4 srd, u32 voff, u32 soff, u32 dst) {
  asm volatile("s_mov_b32 m0, %3\n\ts_nop 0\n\tbuffer_load_dwordx4 %0, %1, %2 offen lds" ::"v"(voff), "s"(srd), "s"(soff), "s"(dst) : "memory");
}
DI void st128(u32x4 v, u32x4 srd, u32 voff, u32 soff) {
  asm volatile("buffer_store_dwordx4 %0, %1, %2, %3 offen sc1\n\ts_nop 1" ::"v"(v), "v"(voff), "s"(srd), "s"(soff) : "memory");
}
DI bf16x8 lds_b128(u32 a) { return *(const LAS bf16x8*)(size_t)a; }
DI bf16x8 lds_tr(u32 a, int o0, int o1) {
  s16x4 lo = __builtin_amdgcn_ds_read_tr16_b64_v4i16((LAS s16x4*)(size_t)(a + o0));
  s16x4 hi = __builtin_amdgcn_ds_read_tr16_b64_v4i16((LAS s16x4*)(size_t)(a + o1));
  return __builtin_shufflevector(lo, hi, 0, 1, 2, 3, 4, 5, 6, 7);
}
DI f32x16 mfma(bf16x8 a, bf16x8 b, f32x16 c) { return __builtin_amdgcn_mfma_f32_32x32x16_bf16(a, b, c, 0, 0, 0); }
DI u32 pk(float a, float b) { f32x2 v = {a, b}; return __builtin_bit_cast(u32, __builtin_convertvector(v, b16x2)); }
DI u32x4 pack8(const f32x16& x, int s) {
  u32x4 r;
  r.x = pk(x[8 * s], x[8 * s + 1]); r.y = pk(x[8 * s + 2], x[8 * s + 3]);
  r.z = pk(x[8 * s + 4], x[8 * s + 5]); r.w = pk(x[8 * s + 6], x[8 * s + 7]);
  return r;
}
DI bf16x8 as_frag(u32x4 v) { return __builtin_bit_cast(bf16x8, v); }
DI float bflo(u32 v) { return __builtin_bit_cast(float, v << 16); }
DI float bfhi(u32 v) { return __builtin_bit_cast(float, v & 0xffff0000u); }
DI float mish(float h) {
  float e = __builtin_amdgcn_exp2f(fminf(h * LOG2E, 40.f));
  float n = e * __builtin_fmaf(0.5f, e, 1.f);
  return h * n * __builtin_amdgcn_rcpf(n + 1.f);
}
DI float phi(float t) { return (fmaxf(t, 0.f) + __builtin_amdgcn_exp2f(fminf(t * LOG2E, 0.f))) + 1e-6f; }

#define ST(n)

struct Params {
  const u16 *x, *wqkv, *wo, *w1, *b1, *w2, *gw;
  u16* out;
  char* ws;
};

DI u32 wait_flags(const char* ws, u32 off, bool active, u32 tag) {
  u64 v = 0;
  for (u32 sp = 0; sp < SPIN_CAP; ++sp) {
    v = __hip_atomic_load((GAS u64*)(ws + off), RLX_AGENT);
    bool ok = !active || ((u32)(v >> 32) == tag && ((u32)v >> 8) == (~tag >> 8));
    if (__all(ok)) break;
    __builtin_amdgcn_s_sleep(2);
  }
  __builtin_amdgcn_fence(__ATOMIC_ACQUIRE, "wavefront");
  return (u32)v & 0xffu;
}
DI void set_flag(char* ws, u32 off, u32 tag, u32 region, int lane) {
  if (lane == 0) __hip_atomic_store((GAS u64*)(ws + off), ((u64)tag << 32) | (u64)((~tag & 0xffffff00u) | region), RLX_AGENT);
}

struct Ctx {
  u32x4 sx, sw;
  u32 vx, vw, vh;
  u32 ox, ou0, ou1, ou2, oh;
  u32 rs64;
  u32x4 s2, so;
  u32 v2, o2;
  int w;
};

template <bool HALF> DI void issue_chunk(const Ctx& c, int ch, u32 slot) {
  const u32 d = slot + c.w * 1024;
  const u32 wo = ch * c.rs64;
  dma(c.sx, c.vx, c.ox + ch * 128, d);
  dma(c.sw, c.vw, c.ou0 + wo, d + L_U);
  dma(c.sx, c.vx, c.ox + ch * 128 + 65536, d + 8192);
  dma(c.sw, c.vw, c.ou1 + wo, d + L_U + 8192);
  dma(c.sw, c.vw, c.ou2 + wo, d + L_U + 16384);
  if (HALF) dma(c.sw, c.vh, c.oh + wo, d + L_HALF - 4096);
}
DI void issue_w2(const Ctx& c, int kl0, u32 slot) {
  const u32 d = slot + c.w * 6144;
#pragma unroll
  for (int q = 0; q < 6; ++q) dma(c.s2, c.v2, c.o2 + kl0 * 16384 + q * 8192, d + q * 1024);
}

template <int ROLE> DI void gemm1(const Ctx& c, f32x16 (&acc)[4], float& gsum, u32 ax, u32 aw0, u32 aw1, u32 aw2, u32 ag) {
  constexpr bool A = ROLE < 2;
  constexpr int NW = ROLE == 2 ? 3 : 2;
  bf16x8 xf[2][2], wf[2][3], gf[2];
  auto rd = [&](int b, u32 slot, int ks) {
    const u32 a = (ax + slot) ^ (ks * 32);
    xf[b][0] = lds_b128(a);
    if (A) xf[b][1] = lds_b128(a + 4096);
    wf[b][0] = lds_tr(aw0 + slot, ks * 2048, ks * 2048 + 512);
    wf[b][1] = lds_tr(aw1 + slot, ks * 2048, ks * 2048 + 512);
    if (NW == 3) wf[b][2] = lds_tr(aw2 + slot, ks * 1024, ks * 1024 + 256);
  };
  auto mm = [&](int b) {
    if (ROLE == 0) {
      acc[0] = mfma(wf[b][0], xf[b][0], acc[0]); acc[1] = mfma(wf[b][0], xf[b][1], acc[1]);
      acc[2] = mfma(wf[b][1], xf[b][0], acc[2]); acc[3] = mfma(wf[b][1], xf[b][1], acc[3]);
    } else if (ROLE == 1) {
      acc[0] = mfma(xf[b][0], wf[b][0], acc[0]); acc[1] = mfma(xf[b][1], wf[b][0], acc[1]);
      acc[2] = mfma(xf[b][0], wf[b][1], acc[2]); acc[3] = mfma(xf[b][1], wf[b][1], acc[3]);
    } else {
      acc[0] = mfma(wf[b][0], xf[b][0], acc[0]); acc[1] = mfma(wf[b][1], xf[b][0], acc[1]);
      if (NW == 3) acc[2] = mfma(wf[b][2], xf[b][0], acc[2]);
      if (ROLE == 3) {
        u32x4 xv = __builtin_bit_cast(u32x4, xf[b][0]), gv = __builtin_bit_cast(u32x4, gf[b]);
#pragma unroll
        for (int q = 0; q < 4; ++q) {
          const u32 xa = xv[q], ga = gv[q];
          gsum = __builtin_amdgcn_fdot2_f32_bf16(__builtin_bit_cast(b16x2, xa), __builtin_bit_cast(b16x2, ga), gsum, false);
        }
      }
    }
  };
  u32 slot = 0;
  rd(0, 0, 0);
  if (ROLE == 3) gf[0] = lds_b128(ag);
  for (int ch = 0; ch < 8; ++ch) {
    const u32 nslot = slot == 2 * SLOT ? 0 : slot + SLOT;
    rd(1, slot, 1); if (ROLE == 3) gf[1] = lds_b128(ag + ch * 128 + 32);
    SB(); mm(0); SB();
    rd(0, slot, 2); if (ROLE == 3) gf[0] = lds_b128(ag + ch * 128 + 64);
    SB(); mm(1); SB();
    rd(1, slot, 3); if (ROLE == 3) gf[1] = lds_b128(ag + ch * 128 + 96);
    SB(); mm(0); SB();
    if (ROLE == 2) asm volatile("s_waitcnt vmcnt(6) lgkmcnt(0)\n\ts_barrier" ::: "memory");
    else asm volatile("s_waitcnt vmcnt(5) lgkmcnt(0)\n\ts_barrier" ::: "memory");
    rd(0, nslot, 0); if (ROLE == 3) gf[0] = lds_b128(ag + ((ch + 1) & 7) * 128);
    SB(); mm(1); SB();
    if (ch < 5) issue_chunk<ROLE == 2>(c, ch + 3, slot);
    else issue_w2(c, (ch - 5) * 3, slot);
    SB();
    slot = nslot;
  }
}

__global__ __launch_bounds__(512, 2) void fused(Params p) {
  extern __shared__ __attribute__((aligned(16))) char smem[];
  const int lane = threadIdx.x & 63, w = __builtin_amdgcn_readfirstlane(threadIdx.x >> 6);
  const int b = blockIdx.x, xcd = b & 7, r = b >> 3;
  const int i1 = (xcd & 1) * 8 + (r & 7), isQ = (r >> 3) & 1, j1 = (xcd >> 1) * 2 + (r >> 4);
  const int tm = (xcd & 3) * 8 + (r & 7), tn = (xcd >> 2) * 4 + (r >> 3);
  const int h = lane >> 5, tok = lane & 31, g1 = (lane >> 4) & 1, q4 = (lane & 15) >> 2, p4 = lane & 3;
  const bool isA = w < 4;
  ST(0);
  Ctx c;
  c.w = w;
  const u32 RS = isQ ? 3072 : 3584;
  c.sx = mk_srd(p.x, 2048 * 1024);
  c.sw = isQ ? mk_srd(p.wqkv, 512 * 3072) : mk_srd(p.w1, 512 * 3584);
  c.s2 = mk_srd(p.w2, 1792 * 1024);
  c.so = mk_srd(p.wo, 512 * 1024);
  const u32 swz = 16 * ((lane & 7) ^ (((lane >> 3) & 2) << 1));
  c.vx = (lane >> 3) * 1024 + 16 * ((lane & 7) ^ ((4 * (w & 1) + (lane >> 4)) & 7));
  c.vw = (lane >> 3) * RS + swz;
  c.vh = (lane >> 2) * RS + 16 * (lane & 3);
  c.v2 = (lane >> 3) * 1024 + swz;
  c.rs64 = 64 * RS;
  const int cU = 224 * j1 + ((j1 & 1) ? 32 : 0), cH = 224 * j1 + ((j1 & 1) ? 0 : 192);
  c.ox = (128 * i1 + 8 * w) * 1024;
  c.ou0 = 8 * w * RS + 2 * (isQ ? 512 + 64 * j1 : cU);
  c.ou1 = 8 * w * RS + 2 * (isQ ? 1024 + 64 * j1 : cU + 64);
  c.ou2 = 8 * w * RS + 2 * (isQ ? 64 * j1 : cU + 128);
  c.oh = 16 * (w & 3) * RS + 2 * cH;
  c.o2 = 224 * w * 1024 + 128 * tn;

  const int t0c = isA ? cU + 64 * (w & 1) : cU + 128, t1c = t0c + 32, t2c = cH;
  u32 epoch;
  u32x2 bb[3][4];
  {
    const u32x4 sw0 = mk_srd(p.ws, 4096), sb = mk_srd(p.b1, 1792 * 2);
    const u32 z = 0;
    asm volatile("buffer_load_dword %0, %1, %2, 0 offen sc1" : "=v"(epoch) : "v"(z), "s"(sw0) : "memory");
    const u32 vb = 8 * h;
    const u32 o0 = 2 * t0c, o1 = 2 * t2c;
#pragma unroll
    for (int q = 0; q < 4; ++q) {
      asm volatile("buffer_load_dwordx2 %0, %1, %2, %3 offen offset:%4" : "=v"(bb[0][q]) : "v"(vb), "s"(sb), "s"(o0), "i"(16 * q) : "memory");
      asm volatile("buffer_load_dwordx2 %0, %1, %2, %3 offen offset:%4" : "=v"(bb[1][q]) : "v"(vb), "s"(sb), "s"(o0), "i"(64 + 16 * q) : "memory");
      asm volatile("buffer_load_dwordx2 %0, %1, %2, %3 offen offset:%4" : "=v"(bb[2][q]) : "v"(vb), "s"(sb), "s"(o1), "i"(16 * q) : "memory");
    }
  }
  if (w == 0) dma(mk_srd(p.gw, 1024), lane * 16, 0, S_GATE);
  const bool half = !isQ && !isA;
  if (half) { issue_chunk<true>(c, 0, 0); issue_chunk<true>(c, 1, SLOT); issue_chunk<true>(c, 2, 2 * SLOT); }
  else { issue_chunk<false>(c, 0, 0); issue_chunk<false>(c, 1, SLOT); issue_chunk<false>(c, 2, 2 * SLOT); }
  if (threadIdx.x == 0) *(LAS u32*)(size_t)S_MISC = (u32)(__builtin_amdgcn_s_memrealtime() >> 9) % NREG;
  asm volatile("s_waitcnt vmcnt(15)"
               : "+v"(epoch), "+v"(bb[0][0]), "+v"(bb[0][1]), "+v"(bb[0][2]), "+v"(bb[0][3]), "+v"(bb[1][0]), "+v"(bb[1][1]), "+v"(bb[1][2]),
                 "+v"(bb[1][3]), "+v"(bb[2][0]), "+v"(bb[2][1]), "+v"(bb[2][2]), "+v"(bb[2][3])::"memory");
  const u32 tag = __builtin_amdgcn_readfirstlane(epoch) + 1;
  f32x16 acc[4];
  {
    f32x16 bt[3];
#pragma unroll
    for (int t = 0; t < 3; ++t)
#pragma unroll
      for (int q = 0; q < 4; ++q) {
        bt[t][4 * q] = isQ ? 0.f : bflo(bb[t][q].x); bt[t][4 * q + 1] = isQ ? 0.f : bfhi(bb[t][q].x);
        bt[t][4 * q + 2] = isQ ? 0.f : bflo(bb[t][q].y); bt[t][4 * q + 3] = isQ ? 0.f : bfhi(bb[t][q].y);
      }
    acc[0] = bt[0]; acc[1] = isA ? bt[0] : bt[1]; acc[2] = isA ? bt[1] : bt[2]; acc[3] = bt[1];
  }
  const u32 trc = 16 * ((2 * g1 + (p4 >> 1)) ^ ((q4 & 2) << 1)) + 8 * (p4 & 1);
  const u32 tr1 = 128 * (8 * h + q4) + trc;
  const u32 ax = 128 * ((isA ? 64 * (w >> 1) : 32 * (w & 3)) + tok) + 16 * (h ^ ((tok >> 1) & 7));
  u32 aw0, aw1;
  if (!isA) { aw0 = L_U + 16384 + tr1; aw1 = aw0 ^ 64; }
  else if (!isQ) { aw0 = L_U + 8192 * (w & 1) + tr1; aw1 = aw0 ^ 64; }
  else { aw0 = L_U + (tr1 ^ (64 * (w & 1))); aw1 = aw0 + 8192; }
  const u32 aw2 = L_HALF + 64 * (8 * h + q4) + 32 * g1 + 8 * p4;
  const u32 ag = S_GATE + 16 * h;
  if (half) asm volatile("s_waitcnt vmcnt(12) lgkmcnt(0)\n\ts_barrier" ::: "memory");
  else asm volatile("s_waitcnt vmcnt(10) lgkmcnt(0)\n\ts_barrier" ::: "memory");
  ST(1);
  const u32 region = __builtin_amdgcn_readfirstlane(*(volatile LAS u32*)(size_t)S_MISC);
  const u32 rbase = W_REG0 + region * R_STRIDE;
  float gsum = 0.f;
  if (isA) { if (isQ) gemm1<1>(c, acc, gsum, ax, aw0, aw1, aw2, ag); else gemm1<0>(c, acc, gsum, ax, aw0, aw1, aw2, ag); }
  else { if (isQ) gemm1<3>(c, acc, gsum, ax, aw0, aw1, aw2, ag); else gemm1<2>(c, acc, gsum, ax, aw0, aw1, aw2, ag); }
  ST(2);
  const __amdgpu_buffer_rsrc_t rws = __builtin_amdgcn_make_buffer_rsrc(p.ws, 0, WS_BYTES, 0x00020000);
  const u32x4 sws = mk_srd(p.ws, WS_BYTES);
  const u32 l16 = lane * 16;
  bf16x8 wr[9][2];
  const u32 tr2 = 128 * (4 * h + q4) + trc + w * 6144;
  auto gen1_gen2 = [&]() {
#pragma unroll
    for (int k = 0; k < 9; ++k) {
      const u32 sl = k < 3 ? 2 * SLOT : k < 6 ? 0 : SLOT;
      wr[k][0] = lds_tr(tr2 + sl, (k % 3) * 2048, (k % 3) * 2048 + 1024);
      wr[k][1] = lds_tr((tr2 ^ 64) + sl, (k % 3) * 2048, (k % 3) * 2048 + 1024);
    }
    asm volatile("s_waitcnt lgkmcnt(0)" ::: "memory");
    SB();
    issue_w2(c, 9, 2 * SLOT);
    {
      const u32 d = w * 6144, oo = 64 * w * 1024 + 128 * tn;
#pragma unroll
      for (int q = 0; q < 4; ++q) dma(c.s2, c.v2, c.o2 + 12 * 16384 + q * 8192, d + q * 1024);
#pragma unroll
      for (int q = 0; q < 2; ++q) dma(c.so, c.v2, oo + q * 8192, d + 4096 + q * 1024);
#pragma unroll
      for (int q = 0; q < 6; ++q) dma(c.so, c.v2, oo + 16384 + q * 8192, SLOT + d + q * 1024);
    }
    SB();
  };
  if (!isQ) {
    const int tt0 = 4 * i1 + (isA ? 2 * (w >> 1) : (w & 3));
#pragma unroll
    for (int t = 0; t < 4; ++t) {
      if (t == 3 && !isA) break;
      const int tt = tt0 + (isA ? (t & 1) : 0);
      const int col = isA ? t0c + 32 * (t >> 1) : (t == 0 ? t0c : t == 1 ? t1c : t2c);
      const u32 o = rbase + (((tt >> 1) * 144 + (col >> 4)) * 2 + (tt & 1)) * 1024;
      f32x16 a;
#pragma unroll
      for (int e = 0; e < 16; ++e) a[e] = mish(acc[t][e]);
      st128(pack8(a, 0), sws, l16, o);
      st128(pack8(a, 1), sws, l16, o + 2048);
    }
    ST(3);
    asm volatile("s_waitcnt vmcnt(0)" ::: "memory");
    set_flag(p.ws, W_FH + ((i1 * 8 + j1) * 8 + w) * 128, tag, region, lane);
    ST(4);
    gen1_gen2();
  } else {
    const int th = w >> 1, pp = w & 1;
    u32x4 pq[4];
    if (isA) {
      f32x16 kv, z;
#pragma unroll
      for (int e = 0; e < 16; ++e) { kv[e] = 0.f; z[e] = 0.f; }
      const u32x4 ones = {0x3f803f80u, 0x3f803f80u, 0x3f803f80u, 0x3f803f80u};
#pragma unroll
      for (int t = 0; t < 2; ++t) {
        f32x16 a;
#pragma unroll
        for (int e = 0; e < 16; ++e) a[e] = phi(acc[t][e]);
#pragma unroll
        for (int s = 0; s < 2; ++s) {
          const bf16x8 kf = as_frag(pack8(a, s));
          kv = mfma(kf, as_frag(pack8(acc[2 + t], s)), kv);
          z = mfma(kf, as_frag(ones), z);
        }
      }
      f32x4 u0, u1, z0, z1;
#pragma unroll
      for (int e = 0; e < 4; ++e) {
        u0[e] = g1 ? kv[8 + e] : kv[e]; u1[e] = g1 ? kv[12 + e] : kv[4 + e];
        z0[e] = g1 ? z[8 + e] : z[e]; z1[e] = g1 ? z[12 + e] : z[4 + e];
      }
      LAS f32x4* red = (LAS f32x4*)(size_t)(S_KVR + pp * 4096 + l16);
      if (th == 1) { red[0] = u0; red[64] = u1; red[128] = z0; red[192] = z1; }
      asm volatile("s_waitcnt lgkmcnt(0)\n\ts_barrier" ::: "memory");
      if (th == 0) {
        u0 += red[0]; u1 += red[64]; z0 += red[128]; z1 += red[192];
        const u32 idx = (j1 * 2 + pp) * 16 + i1;
        st128(__builtin_bit_cast(u32x4, u0), sws, l16, rbase + R_KV + idx * 2048);
        st128(__builtin_bit_cast(u32x4, u1), sws, l16, rbase + R_KV + idx * 2048 + 1024);
        if ((lane & 15) == 0) {
          st128(__builtin_bit_cast(u32x4, z0), sws, (lane >> 4) * 32, rbase + R_Z + idx * 128);
          st128(__builtin_bit_cast(u32x4, z1), sws, (lane >> 4) * 32 + 16, rbase + R_Z + idx * 128);
        }
        asm volatile("s_waitcnt vmcnt(0)" ::: "memory");
        set_flag(p.ws, W_FKV + idx * 128, tag, region, lane);
      }
    } else {
      asm volatile("s_barrier" ::: "memory");
#pragma unroll
      for (int t = 0; t < 2; ++t) {
        f32x16 a;
#pragma unroll
        for (int e = 0; e < 16; ++e) a[e] = phi(acc[t][e]);
        pq[2 * t] = pack8(a, 0); pq[2 * t + 1] = pack8(a, 1);
      }
    }
    ST(3);
    asm volatile("s_waitcnt vmcnt(0)" ::: "memory");
    gen1_gen2();
    asm volatile("s_barrier" ::: "memory");
    if (isA) {
      const u32 idx0 = (j1 * 2 + pp) * 16 + 8 * th;
      const u32 reg = wait_flags(p.ws, W_FKV + (idx0 + (lane & 7)) * 128, true, tag);
      f32x4 u0 = {0, 0, 0, 0}, u1 = u0, z0 = u0, z1 = u0;
#pragma unroll
      for (int q = 0; q < 8; ++q) {
        const u32 rb = W_REG0 + __builtin_amdgcn_readlane(reg, q) * R_STRIDE;
        u0 += __builtin_bit_cast(f32x4, __builtin_amdgcn_raw_buffer_load_b128(rws, l16, rb + R_KV + (idx0 + q) * 2048, 16));
        u1 += __builtin_bit_cast(f32x4, __builtin_amdgcn_raw_buffer_load_b128(rws, l16, rb + R_KV + (idx0 + q) * 2048 + 1024, 16));
        z0 += __builtin_bit_cast(f32x4, __builtin_amdgcn_raw_buffer_load_b128(rws, (lane >> 4) * 32, rb + R_Z + (idx0 + q) * 128, 16));
        z1 += __builtin_bit_cast(f32x4, __builtin_amdgcn_raw_buffer_load_b128(rws, (lane >> 4) * 32 + 16, rb + R_Z + (idx0 + q) * 128, 16));
      }
      LAS f32x4* gk = (LAS f32x4*)(size_t)(S_KVG + w * 2048 + l16);
      gk[0] = u0; gk[64] = u1;
      if ((lane & 15) == 0) {
        LAS f32x4* gz = (LAS f32x4*)(size_t)(S_ZG + w * 128 + (lane >> 4) * 32);
        gz[0] = z0; gz[1] = z1;
      }
      asm volatile("s_waitcnt lgkmcnt(0)\n\ts_barrier" ::: "memory");
    } else {
      asm volatile("s_barrier" ::: "memory");
      const float s = fminf(gsum + __shfl_xor(gsum, 32), 30.f);
      const float es = __builtin_amdgcn_exp2f(s * LOG2E);
      const int tt = 4 * i1 + (w & 3);
#pragma unroll
      for (int pr = 0; pr < 2; ++pr) {
        LAS f32x4* gk = (LAS f32x4*)(size_t)(S_KVG + pr * 2048 + l16);
        LAS f32x4* gz = (LAS f32x4*)(size_t)(S_ZG + pr * 128 + (lane >> 4) * 32);
        const f32x4 u0 = gk[0] + gk[256], u1 = gk[64] + gk[256 + 64], z0 = gz[0] + gz[16], z1 = gz[1] + gz[17];
        const u32x4 kf = {pk(u0[0], u0[1]), pk(u0[2], u0[3]), pk(u1[0], u1[1]), pk(u1[2], u1[3])};
        const u32x4 zf = {pk(z0[0], z0[1]), pk(z0[2], z0[3]), pk(z1[0], z1[1]), pk(z1[2], z1[3])};
        const u32x4 zero = {0, 0, 0, 0};
        f32x16 num, den;
#pragma unroll
        for (int e = 0; e < 16; ++e) { num[e] = 0.f; den[e] = 0.f; }
        num = mfma(as_frag(g1 ? zero : kf), as_frag(pq[2 * pr]), num);
        num = mfma(as_frag(g1 ? kf : zero), as_frag(pq[2 * pr + 1]), num);
        den = mfma(as_frag(g1 ? zero : zf), as_frag(pq[2 * pr]), den);
        den = mfma(as_frag(g1 ? zf : zero), as_frag(pq[2 * pr + 1]), den);
        const float r0 = es * __builtin_amdgcn_rcpf(den[0] + 1e-6f), r1 = es * __builtin_amdgcn_rcpf(den[8] + 1e-6f);
#pragma unroll
        for (int e = 0; e < 8; ++e) { num[e] *= r0; num[8 + e] *= r1; }
        const u32 o = rbase + (((tt >> 1) * 144 + 112 + 4 * j1 + 2 * pr) * 2 + (tt & 1)) * 1024;
        st128(pack8(num, 0), sws, l16, o);
        st128(pack8(num, 1), sws, l16, o + 2048);
      }
      if (lane < 32)
        __builtin_amdgcn_raw_buffer_store_b32(__builtin_bit_cast(u32, __builtin_amdgcn_rcpf(1.f + es)), rws, lane * 4,
                                              rbase + R_SC + ((i1 * 8 + j1) * 128 + 32 * (w & 3)) * 4, 16);
      asm volatile("s_waitcnt vmcnt(0)" ::: "memory");
      set_flag(p.ws, W_FO + ((i1 * 8 + j1) * 4 + (w & 3)) * 128, tag, region, lane);
    }
    ST(4);
  }
  ST(5);
  const int i2 = tm >> 1, th2 = tm & 1;
#pragma unroll
  for (int t = 0; t < 4; ++t)
#pragma unroll
    for (int e = 0; e < 16; ++e) acc[t][e] = 0.f;
  {
    const int pw = (lane & 3) < 2 ? 2 * th2 + (lane & 1) : 4 + 2 * th2 + (lane & 1);
    const u32 reg = wait_flags(p.ws, W_FH + ((i2 * 8 + w) * 8 + pw) * 128, true, tag);
    ST(6);
    const u32 ab = W_REG0 + __builtin_amdgcn_readfirstlane(reg) * R_STRIDE + (tm * 144 + 14 * w) * 2048;
    u32x4 af[14][2];
#pragma unroll
    for (int k = 0; k < 14; ++k) {
      af[k][0] = __builtin_amdgcn_raw_buffer_load_b128(rws, l16, ab + k * 2048, 16);
      af[k][1] = __builtin_amdgcn_raw_buffer_load_b128(rws, l16 + 1024, ab + k * 2048, 16);
      if (k == 9) SB();
    }
#pragma unroll
    for (int k = 0; k < 14; ++k) {
      bf16x8 w0, w1;
      if (k < 9) { w0 = wr[k][0]; w1 = wr[k][1]; }
      else {
        const u32 sl = k < 12 ? 2 * SLOT : 0;
        w0 = lds_tr(tr2 + sl, (k % 3) * 2048, (k % 3) * 2048 + 1024);
        w1 = lds_tr((tr2 ^ 64) + sl, (k % 3) * 2048, (k % 3) * 2048 + 1024);
      }
      acc[0] = mfma(w0, as_frag(af[k][0]), acc[0]); acc[1] = mfma(w0, as_frag(af[k][1]), acc[1]);
      acc[2] = mfma(w1, as_frag(af[k][0]), acc[2]); acc[3] = mfma(w1, as_frag(af[k][1]), acc[3]);
    }
  }
  ST(7);
  {
    const u32 reg = wait_flags(p.ws, W_FO + ((i2 * 8 + w) * 4 + 2 * th2 + (lane & 1)) * 128, true, tag);
    ST(8);
    const u32 rb = W_REG0 + __builtin_amdgcn_readfirstlane(reg) * R_STRIDE;
    const u32 ab = rb + (tm * 144 + 112 + 4 * w) * 2048;
    u32x4 af[4][2];
#pragma unroll
    for (int k = 0; k < 4; ++k) {
      af[k][0] = __builtin_amdgcn_raw_buffer_load_b128(rws, l16, ab + k * 2048, 16);
      af[k][1] = __builtin_amdgcn_raw_buffer_load_b128(rws, l16 + 1024, ab + k * 2048, 16);
    }
    const float sc = __builtin_bit_cast(float, __builtin_amdgcn_raw_buffer_load_b32(rws, (lane >> 3) * 4, rb + R_SC + ((i2 * 8 + w) * 128 + 64 * th2 + 8 * w) * 4, 16));
#pragma unroll
    for (int k = 14; k < 18; ++k) {
      const u32 sl = k < 15 ? 0 : SLOT;
      const bf16x8 w0 = lds_tr(tr2 + sl, (k % 3) * 2048, (k % 3) * 2048 + 1024);
      const bf16x8 w1 = lds_tr((tr2 ^ 64) + sl, (k % 3) * 2048, (k % 3) * 2048 + 1024);
      acc[0] = mfma(w0, as_frag(af[k - 14][0]), acc[0]); acc[1] = mfma(w0, as_frag(af[k - 14][1]), acc[1]);
      acc[2] = mfma(w1, as_frag(af[k - 14][0]), acc[2]); acc[3] = mfma(w1, as_frag(af[k - 14][1]), acc[3]);
    }
    ST(9);
    asm volatile("s_waitcnt lgkmcnt(0)" ::: "memory");
#pragma unroll
    for (int tt = 0; tt < 2; ++tt) {
      const int row = 32 * tt + tok, part = row / 24;
      const u32 base = part * SLOT + w * 6144 + (row - 24 * part) * 256;
#pragma unroll
      for (int nt = 0; nt < 2; ++nt)
#pragma unroll
        for (int q = 0; q < 4; ++q) {
          const f32x16& a = acc[2 * nt + tt];
          const f32x4 v = {a[4 * q], a[4 * q + 1], a[4 * q + 2], a[4 * q + 3]};
          *(LAS f32x4*)(size_t)(base + 16 * ((8 * nt + 2 * q + h) ^ (tok & 7))) = v;
        }
    }
    asm volatile("s_waitcnt lgkmcnt(0)\n\ts_barrier" ::: "memory");
    ST(10);
    {
      const int row = 8 * w + (lane >> 3), part = w / 3;
      const u32 base = part * SLOT + (row - 24 * part) * 256;
      const u32 a0 = base + 16 * ((2 * (lane & 7)) ^ (row & 7)), a1 = a0 ^ 16;
      f32x4 s0 = {0, 0, 0, 0}, s1 = s0;
#pragma unroll
      for (int q = 0; q < 8; ++q) {
        s0 += *(const LAS f32x4*)(size_t)(a0 + q * 6144);
        s1 += *(const LAS f32x4*)(size_t)(a1 + q * 6144);
      }
      s0 *= sc; s1 *= sc;
      const u32x4 o = {pk(s0[0], s0[1]), pk(s0[2], s0[3]), pk(s1[0], s1[1]), pk(s1[2], s1[3])};
      st128(o, mk_srd(p.out, 2048 * 1024), (lane >> 3) * 1024 + 16 * (lane & 7), (64 * tm + 8 * w) * 1024 + 128 * tn);
    }
  }
  if (b == 0 && w == 0) {
    wait_flags(p.ws, W_FH + lane * 1024, true, tag);
    wait_flags(p.ws, W_FH + (64 + lane) * 1024, true, tag);
    if (lane == 0) __hip_atomic_store((GAS u32*)(p.ws + W_EPOCH), tag, RLX_AGENT);
  }
  ST(11);
}

extern "C" void kernel_launch(void* const* d_in, const int* in_sizes, int n_in, void* d_out, int out_size, void* d_ws, size_t ws_size,
                              hipStream_t stream) {
  Params p;
  p.x = (const u16*)d_in[0]; p.wqkv = (const u16*)d_in[1]; p.wo = (const u16*)d_in[2];
  p.w1 = (const u16*)d_in[4]; p.b1 = (const u16*)d_in[5]; p.w2 = (const u16*)d_in[6]; p.gw = (const u16*)d_in[7];
  p.out = (u16*)d_out; p.ws = (char*)d_ws;
  (void)hipFuncSetAttribute((const void*)fused, hipFuncAttributeMaxDynamicSharedMemorySize, LDS_BYTES);
  fused<<<256, 512, LDS_BYTES, stream>>>(p);
}
```

```cpp
#define KK 0
#define SIDE 11
#define SIDEW 0
#define SIDEQ -1
#include <hip/hip_runtime.h>
typedef unsigned short u16;
typedef unsigned u32;
typedef unsigned long long u64;
typedef short bf16x8 __attribute__((ext_vector_type(8)));
typedef short s16x4 __attribute__((ext_vector_type(4)));
typedef float f32x16 __attribute__((ext_vector_type(16)));
typedef float f32x4 __attribute__((ext_vector_type(4)));
typedef float f32x2 __attribute__((ext_vector_type(2)));
typedef __bf16 b16x2 __attribute__((ext_vector_type(2)));
typedef u32 u32x4 __attribute__((ext_vector_type(4)));
typedef u32 u32x2 __attribute__((ext_vector_type(2)));
#define LAS __attribute__((address_space(3)))
#define GAS __attribute__((address_space(1)))
#define DI __device__ __forceinline__
#define RLX_AGENT __ATOMIC_RELAXED, __HIP_MEMORY_SCOPE_AGENT
#define SB() __builtin_amdgcn_sched_barrier(0)

constexpr int SLOT = 49152, SCR = 3 * SLOT, LDS_BYTES = 163840;
constexpr int L_U = 16384, L_HALF = 40960;
constexpr int S_GATE = SCR;
constexpr int S_KVR = SCR + 1024;
constexpr int S_KVG = SCR + 1024;
constexpr int S_ZG = SCR + 9216;
constexpr int S_SCALE = SCR + 10240;
constexpr int S_MISC = SCR + 10752;
constexpr u32 W_EPOCH = 0;
constexpr u32 W_FH = 4096;
constexpr u32 W_FKV = W_FH + 16 * 8 * 8 * 128;
constexpr u32 W_FO = W_FKV + 8 * 2 * 16 * 128;
constexpr u32 W_STAMP = 512 * 1024;
constexpr u32 W_REG0 = 1u << 20;
constexpr u32 R_KV = 9u << 20;
constexpr u32 R_Z = R_KV + 512 * 1024;
constexpr u32 R_SC = R_Z + 32 * 1024;
constexpr u32 R_STRIDE = 10u << 20;
constexpr u32 NREG = 24;
constexpr u32 WS_BYTES = W_REG0 + NREG * R_STRIDE;
constexpr u32 SPIN_CAP = 1u << 20;
constexpr float LOG2E = 1.4426950408889634f;

DI u32x4 mk_srd(const void* p, u32 bytes) {
  u64 a = (u64)p;
  u32x4 r;
  r.x = (u32)a; r.y = (u32)(a >> 32) & 0xffffu; r.z = bytes; r.w = 0x00020000u;
  return r;
}
DI void dma(u32x4 srd, u32 voff, u32 soff, u32 dst) {
  asm volatile("s_mov_b32 m0, %3\n\ts_nop 0\n\tbuffer_load_dwordx4 %0, %1, %2 offen lds" ::"v"(voff), "s"(srd), "s"(soff), "s"(dst) : "memory");
}
DI void st128(u32x4 v, u32x4 srd, u32 voff, u32 soff) {
  asm volatile("buffer_store_dwordx4 %0, %1, %2, %3 offen sc1\n\ts_nop 1" ::"v"(v), "v"(voff), "s"(srd), "s"(soff) : "memory");
}
DI bf16x8 lds_b128(u32 a) { return *(const LAS bf16x8*)(size_t)a; }
DI bf16x8 lds_tr(u32 a, int o0, int o1) {
  s16x4 lo = __builtin_amdgcn_ds_read_tr16_b64_v4i16((LAS s16x4*)(size_t)(a + o0));
  s16x4 hi = __builtin_amdgcn_ds_read_tr16_b64_v4i16((LAS s16x4*)(size_t)(a + o1));
  return __builtin_shufflevector(lo, hi, 0, 1, 2, 3, 4, 5, 6, 7);
}
DI f32x16 mfma(bf16x8 a, bf16x8 b, f32x16 c) { return __builtin_amdgcn_mfma_f32_32x32x16_bf16(a, b, c, 0, 0, 0); }
DI u32 pk(float a, float b) { f32x2 v = {a, b}; return __builtin_bit_cast(u32, __builtin_convertvector(v, b16x2)); }
DI u32x4 pack8(const f32x16& x, int s) {
  u32x4 r;
  r.x = pk(x[8 * s], x[8 * s + 1]); r.y = pk(x[8 * s + 2], x[8 * s + 3]);
  r.z = pk(x[8 * s + 4], x[8 * s + 5]); r.w = pk(x[8 * s + 6], x[8 * s + 7]);
  return r;
}
DI bf16x8 as_frag(u32x4 v) { return __builtin_bit_cast(bf16x8, v); }
DI float bflo(u32 v) { return __builtin_bit_cast(float, v << 16); }
DI float bfhi(u32 v) { return __builtin_bit_cast(float, v & 0xffff0000u); }
DI float mish(float h) {
  float e = __builtin_amdgcn_exp2f(fminf(h * LOG2E, 40.f));
  float n = e * __builtin_fmaf(0.5f, e, 1.f);
  return h * n * __builtin_amdgcn_rcpf(n + 1.f);
}
DI float phi(float t) { return (fmaxf(t, 0.f) + __builtin_amdgcn_exp2f(fminf(t * LOG2E, 0.f))) + 1e-6f; }

#define ST(n) do { if ((n) == 0) side0 = __builtin_amdgcn_s_memrealtime(); if ((n) == SIDE) siden = __builtin_amdgcn_s_memrealtime(); } while (0)

struct Params {
  const u16 *x, *wqkv, *wo, *w1, *b1, *w2, *gw;
  u16* out;
  char* ws;
};

DI u32 wait_flags(const char* ws, u32 off, bool active, u32 tag) {
  u64 v = 0;
  for (u32 sp = 0; sp < SPIN_CAP; ++sp) {
    v = __hip_atomic_load((GAS u64*)(ws + off), RLX_AGENT);
    bool ok = !active || ((u32)(v >> 32) == tag && ((u32)v >> 8) == (~tag >> 8));
    if (__all(ok)) break;
    __builtin_amdgcn_s_sleep(2);
  }
  __builtin_amdgcn_fence(__ATOMIC_ACQUIRE, "wavefront");
  return (u32)v & 0xffu;
}
DI void set_flag(char* ws, u32 off, u32 tag, u32 region, int lane) {
  if (lane == 0) __hip_atomic_store((GAS u64*)(ws + off), ((u64)tag << 32) | (u64)((~tag & 0xffffff00u) | region), RLX_AGENT);
}

struct Ctx {
  u32x4 sx, sw;
  u32 vx, vw, vh;
  u32 ox, ou0, ou1, ou2, oh;
  u32 rs64;
  u32x4 s2, so;
  u32 v2, o2;
  int w;
};

template <bool HALF> DI void issue_chunk(const Ctx& c, int ch, u32 slot) {
  const u32 d = slot + c.w * 1024;
  const u32 wo = ch * c.rs64;
  dma(c.sx, c.vx, c.ox + ch * 128, d);
  dma(c.sw, c.vw, c.ou0 + wo, d + L_U);
  dma(c.sx, c.vx, c.ox + ch * 128 + 65536, d + 8192);
  dma(c.sw, c.vw, c.ou1 + wo, d + L_U + 8192);
  dma(c.sw, c.vw, c.ou2 + wo, d + L_U + 16384);
  if (HALF) dma(c.sw, c.vh, c.oh + wo, d + L_HALF - 4096);
}
DI void issue_w2(const Ctx& c, int kl0, u32 slot) {
  const u32 d = slot + c.w * 6144;
#pragma unroll
  for (int q = 0; q < 6; ++q) dma(c.s2, c.v2, c.o2 + kl0 * 16384 + q * 8192, d + q * 1024);
}

template <int ROLE> DI void gemm1(const Ctx& c, f32x16 (&acc)[4], float& gsum, u32 ax, u32 aw0, u32 aw1, u32 aw2, u32 ag) {
  constexpr bool A = ROLE < 2;
  constexpr int NW = ROLE == 2 ? 3 : 2;
  bf16x8 xf[2][2], wf[2][3], gf[2];
  auto rd = [&](int b, u32 slot, int ks) {
    const u32 a = (ax + slot) ^ (ks * 32);
    xf[b][0] = lds_b128(a);
    if (A) xf[b][1] = lds_b128(a + 4096);
    wf[b][0] = lds_tr(aw0 + slot, ks * 2048, ks * 2048 + 512);
    wf[b][1] = lds_tr(aw1 + slot, ks * 2048, ks * 2048 + 512);
    if (NW == 3) wf[b][2] = lds_tr(aw2 + slot, ks * 1024, ks * 1024 + 256);
  };
  auto mm = [&](int b) {
    if (ROLE == 0) {
      acc[0] = mfma(wf[b][0], xf[b][0], acc[0]); acc[1] = mfma(wf[b][0], xf[b][1], acc[1]);
      acc[2] = mfma(wf[b][1], xf[b][0], acc[2]); acc[3] = mfma(wf[b][1], xf[b][1], acc[3]);
    } else if (ROLE == 1) {
      acc[0] = mfma(xf[b][0], wf[b][0], acc[0]); acc[1] = mfma(xf[b][1], wf[b][0], acc[1]);
      acc[2] = mfma(xf[b][0], wf[b][1], acc[2]); acc[3] = mfma(xf[b][1], wf[b][1], acc[3]);
    } else {
      acc[0] = mfma(wf[b][0], xf[b][0], acc[0]); acc[1] = mfma(wf[b][1], xf[b][0], acc[1]);
      if (NW == 3) acc[2] = mfma(wf[b][2], xf[b][0], acc[2]);
      if (ROLE == 3) {
        u32x4 xv = __builtin_bit_cast(u32x4, xf[b][0]), gv = __builtin_bit_cast(u32x4, gf[b]);
#pragma unroll
        for (int q = 0; q < 4; ++q) {
          const u32 xa = xv[q], ga = gv[q];
          gsum = __builtin_amdgcn_fdot2_f32_bf16(__builtin_bit_cast(b16x2, xa), __builtin_bit_cast(b16x2, ga), gsum, false);
        }
      }
    }
  };
  u32 slot = 0;
  rd(0, 0, 0);
  if (ROLE == 3) gf[0] = lds_b128(ag);
  for (int ch = 0; ch < 8; ++ch) {
    const u32 nslot = slot == 2 * SLOT ? 0 : slot + SLOT;
    rd(1, slot, 1); if (ROLE == 3) gf[1] = lds_b128(ag + ch * 128 + 32);
    SB(); mm(0); SB();
    rd(0, slot, 2); if (ROLE == 3) gf[0] = lds_b128(ag + ch * 128 + 64);
    SB(); mm(1); SB();
    rd(1, slot, 3); if (ROLE == 3) gf[1] = lds_b128(ag + ch * 128 + 96);
    SB(); mm(0); SB();
    if (ROLE == 2) asm volatile("s_waitcnt vmcnt(6) lgkmcnt(0)\n\ts_barrier" ::: "memory");
    else asm volatile("s_waitcnt vmcnt(5) lgkmcnt(0)\n\ts_barrier" ::: "memory");
    rd(0, nslot, 0); if (ROLE == 3) gf[0] = lds_b128(ag + ((ch + 1) & 7) * 128);
    SB(); mm(1); SB();
    if (ch < 5) issue_chunk<ROLE == 2>(c, ch + 3, slot);
    else issue_w2(c, (ch - 5) * 3, slot);
    SB();
    slot = nslot;
  }
}

__global__ __launch_bounds__(512, 2) void fused(Params p) {
  extern __shared__ __attribute__((aligned(16))) char smem[];
  const int lane = threadIdx.x & 63, w = __builtin_amdgcn_readfirstlane(threadIdx.x >> 6);
  const int b = blockIdx.x, xcd = b & 7, r = b >> 3;
  const int i1 = (xcd & 1) * 8 + (r & 7), isQ = (r >> 3) & 1, j1 = (xcd >> 1) * 2 + (r >> 4);
  const int tm = (xcd & 3) * 8 + (r & 7), tn = (xcd >> 2) * 4 + (r >> 3);
  const int h = lane >> 5, tok = lane & 31, g1 = (lane >> 4) & 1, q4 = (lane & 15) >> 2, p4 = lane & 3;
  const bool isA = w < 4;
  u64 side0 = 0, siden = 0;
  ST(0);
  if (threadIdx.x == 0) __hip_atomic_store((GAS u64*)(p.ws + W_STAMP + b * 128), side0, RLX_AGENT);
  Ctx c;
  c.w = w;
  const u32 RS = isQ ? 3072 : 3584;
  c.sx = mk_srd(p.x, 2048 * 1024);
  c.sw = isQ ? mk_srd(p.wqkv, 512 * 3072) : mk_srd(p.w1, 512 * 3584);
  c.s2 = mk_srd(p.w2, 1792 * 1024);
  c.so = mk_srd(p.wo, 512 * 1024);
  const u32 swz = 16 * ((lane & 7) ^ (((lane >> 3) & 2) << 1));
  c.vx = (lane >> 3) * 1024 + 16 * ((lane & 7) ^ ((4 * (w & 1) + (lane >> 4)) & 7));
  c.vw = (lane >> 3) * RS + swz;
  c.vh = (lane >> 2) * RS + 16 * (lane & 3);
  c.v2 = (lane >> 3) * 1024 + swz;
  c.rs64 = 64 * RS;
  const int cU = 224 * j1 + ((j1 & 1) ? 32 : 0), cH = 224 * j1 + ((j1 & 1) ? 0 : 192);
  c.ox = (128 * i1 + 8 * w) * 1024;
  c.ou0 = 8 * w * RS + 2 * (isQ ? 512 + 64 * j1 : cU);
  c.ou1 = 8 * w * RS + 2 * (isQ ? 1024 + 64 * j1 : cU + 64);
  c.ou2 = 8 * w * RS + 2 * (isQ ? 64 * j1 : cU + 128);
  c.oh = 16 * (w & 3) * RS + 2 * cH;
  c.o2 = 224 * w * 1024 + 128 * tn;

  const int t0c = isA ? cU + 64 * (w & 1) : cU + 128, t1c = t0c + 32, t2c = cH;
  u32 epoch;
  u32x2 bb[3][4];
  {
    const u32x4 sw0 = mk_srd(p.ws, 4096), sb = mk_srd(p.b1, 1792 * 2);
    const u32 z = 0;
    asm volatile("buffer_load_dword %0, %1, %2, 0 offen sc1" : "=v"(epoch) : "v"(z), "s"(sw0) : "memory");
    const u32 vb = 8 * h;
    const u32 o0 = 2 * t0c, o1 = 2 * t2c;
#pragma unroll
    for (int q = 0; q < 4; ++q) {
      asm volatile("buffer_load_dwordx2 %0, %1, %2, %3 offen offset:%4" : "=v"(bb[0][q]) : "v"(vb), "s"(sb), "s"(o0), "i"(16 * q) : "memory");
      asm volatile("buffer_load_dwordx2 %0, %1, %2, %3 offen offset:%4" : "=v"(bb[1][q]) : "v"(vb), "s"(sb), "s"(o0), "i"(64 + 16 * q) : "memory");
      asm volatile("buffer_load_dwordx2 %0, %1, %2, %3 offen offset:%4" : "=v"(bb[2][q]) : "v"(vb), "s"(sb), "s"(o1), "i"(16 * q) : "memory");
    }
  }
  if (w == 0) dma(mk_srd(p.gw, 1024), lane * 16, 0, S_GATE);
  const bool half = !isQ && !isA;
  if (half) { issue_chunk<true>(c, 0, 0); issue_chunk<true>(c, 1, SLOT); issue_chunk<true>(c, 2, 2 * SLOT); }
  else { issue_chunk<false>(c, 0, 0); issue_chunk<false>(c, 1, SLOT); issue_chunk<false>(c, 2, 2 * SLOT); }
  if (threadIdx.x == 0) *(LAS u32*)(size_t)S_MISC = (u32)(__builtin_amdgcn_s_memrealtime() >> 9) % NREG;
  asm volatile("s_waitcnt vmcnt(15)"
               : "+v"(epoch), "+v"(bb[0][0]), "+v"(bb[0][1]), "+v"(bb[0][2]), "+v"(bb[0][3]), "+v"(bb[1][0]), "+v"(bb[1][1]), "+v"(bb[1][2]),
                 "+v"(bb[1][3]), "+v"(bb[2][0]), "+v"(bb[2][1]), "+v"(bb[2][2]), "+v"(bb[2][3])::"memory");
  const u32 tag = __builtin_amdgcn_readfirstlane(epoch) + 1;
  f32x16 acc[4];
  {
    f32x16 bt[3];
#pragma unroll
    for (int t = 0; t < 3; ++t)
#pragma unroll
      for (int q = 0; q < 4; ++q) {
        bt[t][4 * q] = isQ ? 0.f : bflo(bb[t][q].x); bt[t][4 * q + 1] = isQ ? 0.f : bfhi(bb[t][q].x);
        bt[t][4 * q + 2] = isQ ? 0.f : bflo(bb[t][q].y); bt[t][4 * q + 3] = isQ ? 0.f : bfhi(bb[t][q].y);
      }
    acc[0] = bt[0]; acc[1] = isA ? bt[0] : bt[1]; acc[2] = isA ? bt[1] : bt[2]; acc[3] = bt[1];
  }
  const u32 trc = 16 * ((2 * g1 + (p4 >> 1)) ^ ((q4 & 2) << 1)) + 8 * (p4 & 1);
  const u32 tr1 = 128 * (8 * h + q4) + trc;
  const u32 ax = 128 * ((isA ? 64 * (w >> 1) : 32 * (w & 3)) + tok) + 16 * (h ^ ((tok >> 1) & 7));
  u32 aw0, aw1;
  if (!isA) { aw0 = L_U + 16384 + tr1; aw1 = aw0 ^ 64; }
  else if (!isQ) { aw0 = L_U + 8192 * (w & 1) + tr1; aw1 = aw0 ^ 64; }
  else { aw0 = L_U + (tr1 ^ (64 * (w & 1))); aw1 = aw0 + 8192; }
  const u32 aw2 = L_HALF + 64 * (8 * h + q4) + 32 * g1 + 8 * p4;
  const u32 ag = S_GATE + 16 * h;
  if (half) asm volatile("s_waitcnt vmcnt(12) lgkmcnt(0)\n\ts_barrier" ::: "memory");
  else asm volatile("s_waitcnt vmcnt(10) lgkmcnt(0)\n\ts_barrier" ::: "memory");
  ST(1);
  const u32 region = __builtin_amdgcn_readfirstlane(*(volatile LAS u32*)(size_t)S_MISC);
  const u32 rbase = W_REG0 + region * R_STRIDE;
  float gsum = 0.f;
  if (isA) { if (isQ) gemm1<1>(c, acc, gsum, ax, aw0, aw1, aw2, ag); else gemm1<0>(c, acc, gsum, ax, aw0, aw1, aw2, ag); }
  else { if (isQ) gemm1<3>(c, acc, gsum, ax, aw0, aw1, aw2, ag); else gemm1<2>(c, acc, gsum, ax, aw0, aw1, aw2, ag); }
  ST(2);
  const __amdgpu_buffer_rsrc_t rws = __builtin_amdgcn_make_buffer_rsrc(p.ws, 0, WS_BYTES, 0x00020000);
  const u32x4 sws = mk_srd(p.ws, WS_BYTES);
  const u32 l16 = lane * 16;
  bf16x8 wr[9][2];
  const u32 tr2 = 128 * (4 * h + q4) + trc + w * 6144;
  auto gen1_gen2 = [&]() {
#pragma unroll
    for (int k = 0; k < 9; ++k) {
      const u32 sl = k < 3 ? 2 * SLOT : k < 6 ? 0 : SLOT;
      wr[k][0] = lds_tr(tr2 + sl, (k % 3) * 2048, (k % 3) * 2048 + 1024);
      wr[k][1] = lds_tr((tr2 ^ 64) + sl, (k % 3) * 2048, (k % 3) * 2048 + 1024);
    }
    asm volatile("s_waitcnt lgkmcnt(0)" ::: "memory");
    SB();
    issue_w2(c, 9, 2 * SLOT);
    {
      const u32 d = w * 6144, oo = 64 * w * 1024 + 128 * tn;
#pragma unroll
      for (int q = 0; q < 4; ++q) dma(c.s2, c.v2, c.o2 + 12 * 16384 + q * 8192, d + q * 1024);
#pragma unroll
      for (int q = 0; q < 2; ++q) dma(c.so, c.v2, oo + q * 8192, d + 4096 + q * 1024);
#pragma unroll
      for (int q = 0; q < 6; ++q) dma(c.so, c.v2, oo + 16384 + q * 8192, SLOT + d + q * 1024);
    }
    SB();
  };
  if (!isQ) {
    const int tt0 = 4 * i1 + (isA ? 2 * (w >> 1) : (w & 3));
#pragma unroll
    for (int t = 0; t < 4; ++t) {
      if (t == 3 && !isA) break;
      const int tt = tt0 + (isA ? (t & 1) : 0);
      const int col = isA ? t0c + 32 * (t >> 1) : (t == 0 ? t0c : t == 1 ? t1c : t2c);
      const u32 o = rbase + (((tt >> 1) * 144 + (col >> 4)) * 2 + (tt & 1)) * 1024;
      f32x16 a;
#pragma unroll
      for (int e = 0; e < 16; ++e) a[e] = mish(acc[t][e]);
      st128(pack8(a, 0), sws, l16, o);
      st128(pack8(a, 1), sws, l16, o + 2048);
    }
    ST(3);
    asm volatile("s_waitcnt vmcnt(0)" ::: "memory");
    set_flag(p.ws, W_FH + ((i1 * 8 + j1) * 8 + w) * 128, tag, region, lane);
    ST(4);
    gen1_gen2();
  } else {
    const int th = w >> 1, pp = w & 1;
    u32x4 pq[4];
    if (isA) {
      f32x16 kv, z;
#pragma unroll
      for (int e = 0; e < 16; ++e) { kv[e] = 0.f; z[e] = 0.f; }
      const u32x4 ones = {0x3f803f80u, 0x3f803f80u, 0x3f803f80u, 0x3f803f80u};
#pragma unroll
      for (int t = 0; t < 2; ++t) {
        f32x16 a;
#pragma unroll
        for (int e = 0; e < 16; ++e) a[e] = phi(acc[t][e]);
#pragma unroll
        for (int s = 0; s < 2; ++s) {
          const bf16x8 kf = as_frag(pack8(a, s));
          kv = mfma(kf, as_frag(pack8(acc[2 + t], s)), kv);
          z = mfma(kf, as_frag(ones), z);
        }
      }
      f32x4 u0, u1, z0, z1;
#pragma unroll
      for (int e = 0; e < 4; ++e) {
        u0[e] = g1 ? kv[8 + e] : kv[e]; u1[e] = g1 ? kv[12 + e] : kv[4 + e];
        z0[e] = g1 ? z[8 + e] : z[e]; z1[e] = g1 ? z[12 + e] : z[4 + e];
      }
      LAS f32x4* red = (LAS f32x4*)(size_t)(S_KVR + pp * 4096 + l16);
      if (th == 1) { red[0] = u0; red[64] = u1; red[128] = z0; red[192] = z1; }
      asm volatile("s_waitcnt lgkmcnt(0)\n\ts_barrier" ::: "memory");
      if (th == 0) {
        u0 += red[0]; u1 += red[64]; z0 += red[128]; z1 += red[192];
        const u32 idx = (j1 * 2 + pp) * 16 + i1;
        st128(__builtin_bit_cast(u32x4, u0), sws, l16, rbase + R_KV + idx * 2048);
        st128(__builtin_bit_cast(u32x4, u1), sws, l16, rbase + R_KV + idx * 2048 + 1024);
        if ((lane & 15) == 0) {
          st128(__builtin_bit_cast(u32x4, z0), sws, (lane >> 4) * 32, rbase + R_Z + idx * 128);
          st128(__builtin_bit_cast(u32x4, z1), sws, (lane >> 4) * 32 + 16, rbase + R_Z + idx * 128);
        }
        asm volatile("s_waitcnt vmcnt(0)" ::: "memory");
        set_flag(p.ws, W_FKV + idx * 128, tag, region, lane);
      }
    } else {
      asm volatile("s_barrier" ::: "memory");
#pragma unroll
      for (int t = 0; t < 2; ++t) {
        f32x16 a;
#pragma unroll
        for (int e = 0; e < 16; ++e) a[e] = phi(acc[t][e]);
        pq[2 * t] = pack8(a, 0); pq[2 * t + 1] = pack8(a, 1);
      }
    }
    ST(3);
    asm volatile("s_waitcnt vmcnt(0)" ::: "memory");
    gen1_gen2();
    asm volatile("s_barrier" ::: "memory");
    if (isA) {
      const u32 idx0 = (j1 * 2 + pp) * 16 + 8 * th;
      const u32 reg = wait_flags(p.ws, W_FKV + (idx0 + (lane & 7)) * 128, true, tag);
      f32x4 u0 = {0, 0, 0, 0}, u1 = u0, z0 = u0, z1 = u0;
#pragma unroll
      for (int q = 0; q < 8; ++q) {
        const u32 rb = W_REG0 + __builtin_amdgcn_readlane(reg, q) * R_STRIDE;
        u0 += __builtin_bit_cast(f32x4, __builtin_amdgcn_raw_buffer_load_b128(rws, l16, rb + R_KV + (idx0 + q) * 2048, 16));
        u1 += __builtin_bit_cast(f32x4, __builtin_amdgcn_raw_buffer_load_b128(rws, l16, rb + R_KV + (idx0 + q) * 2048 + 1024, 16));
        z0 += __builtin_bit_cast(f32x4, __builtin_amdgcn_raw_buffer_load_b128(rws, (lane >> 4) * 32, rb + R_Z + (idx0 + q) * 128, 16));
        z1 += __builtin_bit_cast(f32x4, __builtin_amdgcn_raw_buffer_load_b128(rws, (lane >> 4) * 32 + 16, rb + R_Z + (idx0 + q) * 128, 16));
      }
      LAS f32x4* gk = (LAS f32x4*)(size_t)(S_KVG + w * 2048 + l16);
      gk[0] = u0; gk[64] = u1;
      if ((lane & 15) == 0) {
        LAS f32x4* gz = (LAS f32x4*)(size_t)(S_ZG + w * 128 + (lane >> 4) * 32);
        gz[0] = z0; gz[1] = z1;
      }
      asm volatile("s_waitcnt lgkmcnt(0)\n\ts_barrier" ::: "memory");
    } else {
      asm volatile("s_barrier" ::: "memory");
      const float s = fminf(gsum + __shfl_xor(gsum, 32), 30.f);
      const float es = __builtin_amdgcn_exp2f(s * LOG2E);
      const int tt = 4 * i1 + (w & 3);
#pragma unroll
      for (int pr = 0; pr < 2; ++pr) {
        LAS f32x4* gk = (LAS f32x4*)(size_t)(S_KVG + pr * 2048 + l16);
        LAS f32x4* gz = (LAS f32x4*)(size_t)(S_ZG + pr * 128 + (lane >> 4) * 32);
        const f32x4 u0 = gk[0] + gk[256], u1 = gk[64] + gk[256 + 64], z0 = gz[0] + gz[16], z1 = gz[1] + gz[17];
        const u32x4 kf = {pk(u0[0], u0[1]), pk(u0[2], u0[3]), pk(u1[0], u1[1]), pk(u1[2], u1[3])};
        const u32x4 zf = {pk(z0[0], z0[1]), pk(z0[2], z0[3]), pk(z1[0], z1[1]), pk(z1[2], z1[3])};
        const u32x4 zero = {0, 0, 0, 0};
        f32x16 num, den;
#pragma unroll
        for (int e = 0; e < 16; ++e) { num[e] = 0.f; den[e] = 0.f; }
        num = mfma(as_frag(g1 ? zero : kf), as_frag(pq[2 * pr]), num);
        num = mfma(as_frag(g1 ? kf : zero), as_frag(pq[2 * pr + 1]), num);
        den = mfma(as_frag(g1 ? zero : zf), as_frag(pq[2 * pr]), den);
        den = mfma(as_frag(g1 ? zf : zero), as_frag(pq[2 * pr + 1]), den);
        const float r0 = es * __builtin_amdgcn_rcpf(den[0] + 1e-6f), r1 = es * __builtin_amdgcn_rcpf(den[8] + 1e-6f);
#pragma unroll
        for (int e = 0; e < 8; ++e) { num[e] *= r0; num[8 + e] *= r1; }
        const u32 o = rbase + (((tt >> 1) * 144 + 112 + 4 * j1 + 2 * pr) * 2 + (tt & 1)) * 1024;
        st128(pack8(num, 0), sws, l16, o);
        st128(pack8(num, 1), sws, l16, o + 2048);
      }
      if (lane < 32)
        __builtin_amdgcn_raw_buffer_store_b32(__builtin_bit_cast(u32, __builtin_amdgcn_rcpf(1.f + es)), rws, lane * 4,
                                              rbase + R_SC + ((i1 * 8 + j1) * 128 + 32 * (w & 3)) * 4, 16);
      asm volatile("s_waitcnt vmcnt(0)" ::: "memory");
      set_flag(p.ws, W_FO + ((i1 * 8 + j1) * 4 + (w & 3)) * 128, tag, region, lane);
    }
    ST(4);
  }
  ST(5);
  const int i2 = tm >> 1, th2 = tm & 1;
#pragma unroll
  for (int t = 0; t < 4; ++t)
#pragma unroll
    for (int e = 0; e < 16; ++e) acc[t][e] = 0.f;
  {
    const int pw = (lane & 3) < 2 ? 2 * th2 + (lane & 1) : 4 + 2 * th2 + (lane & 1);
    const u32 reg = wait_flags(p.ws, W_FH + ((i2 * 8 + w) * 8 + pw) * 128, true, tag);
    ST(6);
    const u32 ab = W_REG0 + __builtin_amdgcn_readfirstlane(reg) * R_STRIDE + (tm * 144 + 14 * w) * 2048;
    u32x4 af[14][2];
#pragma unroll
    for (int k = 0; k < 14; ++k) {
      af[k][0] = __builtin_amdgcn_raw_buffer_load_b128(rws, l16, ab + k * 2048, 16);
      af[k][1] = __builtin_amdgcn_raw_buffer_load_b128(rws, l16 + 1024, ab + k * 2048, 16);
      if (k == 9) SB();
    }
#pragma unroll
    for (int k = 0; k < 14; ++k) {
      bf16x8 w0, w1;
      if (k < 9) { w0 = wr[k][0]; w1 = wr[k][1]; }
      else {
        const u32 sl = k < 12 ? 2 * SLOT : 0;
        w0 = lds_tr(tr2 + sl, (k % 3) * 2048, (k % 3) * 2048 + 1024);
        w1 = lds_tr((tr2 ^ 64) + sl, (k % 3) * 2048, (k % 3) * 2048 + 1024);
      }
      acc[0] = mfma(w0, as_frag(af[k][0]), acc[0]); acc[1] = mfma(w0, as_frag(af[k][1]), acc[1]);
      acc[2] = mfma(w1, as_frag(af[k][0]), acc[2]); acc[3] = mfma(w1, as_frag(af[k][1]), acc[3]);
    }
  }
  ST(7);
  {
    const u32 reg = wait_flags(p.ws, W_FO + ((i2 * 8 + w) * 4 + 2 * th2 + (lane & 1)) * 128, true, tag);
    ST(8);
    const u32 rb = W_REG0 + __builtin_amdgcn_readfirstlane(reg) * R_STRIDE;
    const u32 ab = rb + (tm * 144 + 112 + 4 * w) * 2048;
    u32x4 af[4][2];
#pragma unroll
    for (int k = 0; k < 4; ++k) {
      af[k][0] = __builtin_amdgcn_raw_buffer_load_b128(rws, l16, ab + k * 2048, 16);
      af[k][1] = __builtin_amdgcn_raw_buffer_load_b128(rws, l16 + 1024, ab + k * 2048, 16);
    }
    const float sc = __builtin_bit_cast(float, __builtin_amdgcn_raw_buffer_load_b32(rws, (lane >> 3) * 4, rb + R_SC + ((i2 * 8 + w) * 128 + 64 * th2 + 8 * w) * 4, 16));
#pragma unroll
    for (int k = 14; k < 18; ++k) {
      const u32 sl = k < 15 ? 0 : SLOT;
      const bf16x8 w0 = lds_tr(tr2 + sl, (k % 3) * 2048, (k % 3) * 2048 + 1024);
      const bf16x8 w1 = lds_tr((tr2 ^ 64) + sl, (k % 3) * 2048, (k % 3) * 2048 + 1024);
      acc[0] = mfma(w0, as_frag(af[k - 14][0]), acc[0]); acc[1] = mfma(w0, as_frag(af[k - 14][1]), acc[1]);
      acc[2] = mfma(w1, as_frag(af[k - 14][0]), acc[2]); acc[3] = mfma(w1, as_frag(af[k - 14][1]), acc[3]);
    }
    ST(9);
    asm volatile("s_waitcnt lgkmcnt(0)" ::: "memory");
#pragma unroll
    for (int tt = 0; tt < 2; ++tt) {
      const int row = 32 * tt + tok, part = row / 24;
      const u32 base = part * SLOT + w * 6144 + (row - 24 * part) * 256;
#pragma unroll
      for (int nt = 0; nt < 2; ++nt)
#pragma unroll
        for (int q = 0; q < 4; ++q) {
          const f32x16& a = acc[2 * nt + tt];
          const f32x4 v = {a[4 * q], a[4 * q + 1], a[4 * q + 2], a[4 * q + 3]};
          *(LAS f32x4*)(size_t)(base + 16 * ((8 * nt + 2 * q + h) ^ (tok & 7))) = v;
        }
    }
    asm volatile("s_waitcnt lgkmcnt(0)\n\ts_barrier" ::: "memory");
    ST(10);
    {
      const int row = 8 * w + (lane >> 3), part = w / 3;
      const u32 base = part * SLOT + (row - 24 * part) * 256;
      const u32 a0 = base + 16 * ((2 * (lane & 7)) ^ (row & 7)), a1 = a0 ^ 16;
      f32x4 s0 = {0, 0, 0, 0}, s1 = s0;
#pragma unroll
      for (int q = 0; q < 8; ++q) {
        s0 += *(const LAS f32x4*)(size_t)(a0 + q * 6144);
        s1 += *(const LAS f32x4*)(size_t)(a1 + q * 6144);
      }
      s0 *= sc; s1 *= sc;
      const u32x4 o = {pk(s0[0], s0[1]), pk(s0[2], s0[3]), pk(s1[0], s1[1]), pk(s1[2], s1[3])};
      *(GAS u32x4*)((GAS char*)p.out + (size_t)(64 * tm + row) * 1024 + 128 * tn + 16 * (lane & 7)) = o;
    }
  }
  if (b == 0 && w == 0) {
    wait_flags(p.ws, W_FH + lane * 1024, true, tag);
    wait_flags(p.ws, W_FH + (64 + lane) * 1024, true, tag);
    if (lane == 0) __hip_atomic_store((GAS u32*)(p.ws + W_EPOCH), tag, RLX_AGENT);
  }
  ST(11);
  if (w == 0) { u64 mx = 0, mn = ~0ull;
    for (int i = 0; i < 4; ++i) { u64 v = __hip_atomic_load((GAS u64*)(p.ws + W_STAMP + (lane * 4 + i) * 128), RLX_AGENT); mx = v > mx ? v : mx; mn = v < mn ? v : mn; }
    for (int o = 32; o > 0; o >>= 1) { u64 v = __shfl_xor(mx, o); mx = v > mx ? v : mx; v = __shfl_xor(mn, o); mn = v < mn ? v : mn; }
    u64 d_ = mx - mn; if (d_ > 5000) d_ = 5000; u64 te = __builtin_amdgcn_s_memrealtime() + KK * d_; while (__builtin_amdgcn_s_memrealtime() < te) __builtin_amdgcn_s_sleep(1); }
}

extern "C" void kernel_launch(void* const* d_in, const int* in_sizes, int n_in, void* d_out, int out_size, void* d_ws, size_t ws_size,
                              hipStream_t stream) {
  Params p;
  p.x = (const u16*)d_in[0]; p.wqkv = (const u16*)d_in[1]; p.wo = (const u16*)d_in[2];
  p.w1 = (const u16*)d_in[4]; p.b1 = (const u16*)d_in[5]; p.w2 = (const u16*)d_in[6]; p.gw = (const u16*)d_in[7];
  p.out = (u16*)d_out; p.ws = (char*)d_ws;
  (void)hipFuncSetAttribute((const void*)fused, hipFuncAttributeMaxDynamicSharedMemorySize, LDS_BYTES);
  fused<<<256, 512, LDS_BYTES, stream>>>(p);
}
```

```cpp
#include <hip/hip_runtime.h>
typedef unsigned u32;
typedef unsigned long long u64;
constexpr u32 W_STAMP = 512 * 1024, W_REG0 = 1u << 20, R_KV = 9u << 20, R_SC = R_KV + 544 * 1024;
constexpr int LDS_BYTES = 163840;
struct Srd { const void* p; u32 bytes; u32 fmt; };
struct Params { Srd x, w2, wo, ws, out, w1, b1, wqkv, gw; };
__global__ __launch_bounds__(512, 2) void fused(Params prm) {
  asm volatile(
      ".Lstart_%=:\n"
      "s_load_dwordx16 s[4:19], s[0:1], 0x0\n"
      "s_bfe_u32 s33, s2, 0x10006\n"
      "s_lshl_b32 s62, s33, 5\n"
      "s_add_u32 s62, s62, 80\n"
      "s_load_dwordx8 s[24:31], s[0:1], s62\n"
      "s_load_dwordx4 s[20:23], s[0:1], 0x40\n"
      "v_readfirstlane_b32 s32, v0\n"
      "v_and_b32 v0, 63, v0\n"
      "s_lshr_b32 s32, s32, 6\n"
      "s_and_b32 s99, s32, 3\n"
      "s_bfe_u32 s63, s2, 0x30003\n"
      "s_and_b32 s64, s2, 1\n"
      "s_lshl3_add_u32 s34, s64, s63\n"
      "s_and_b32 s64, s2, 6\n"
      "s_lshr_b32 s65, s2, 7\n"
      "s_add_u32 s35, s64, s65\n"
      "s_mul_i32 s73, s35, 224\n"
      "s_lshl_b32 s64, s65, 5\n"
      "s_add_u32 s72, s73, s64\n"
      "s_cmp_eq_u32 s65, 0\n"
      "s_cselect_b32 s64, 192, 0\n"
      "s_add_u32 s73, s73, s64\n"
      "s_andn2_b32 s45, 4, s2\n"
      "s_lshr_b32 s45, s45, 1\n"
      "v_lshrrev_b32 v9, 3, v0\n"
      "v_and_b32 v10, 7, v0\n"
      "v_lshrrev_b32 v11, 2, v0\n"
      "v_and_b32 v12, 4, v11\n"
      "v_xor_b32 v12, v12, v10\n"
      "v_lshlrev_b32 v12, 4, v12\n"
      "v_lshl_or_b32 v4, v9, 10, v12\n"
      "s_cmp_lt_u32 s32, 4\n"
      "s_cbranch_scc1 .LA0_2_%=\n"
      "s_lshl_b32 s64, s35, 7\n"
      "s_lshl_b32 s65, s72, 1\n"
      "s_cmp_eq_u32 s33, 0\n"
      "s_cselect_b32 s38, 7, 6\n"
      "s_cselect_b32 s64, s65, s64\n"
      "s_cselect_b32 s41, 0, 1024\n"
      "s_cselect_b32 s42, 1, 16\n"
      "s_cselect_b32 s43, 256, 0\n"
      "s_lshl_b32 s38, s38, 9\n"
      "s_lshl_b32 s42, s42, 7\n"
      "s_lshl_b32 s65, s99, 3\n"
      "s_mul_i32 s65, s65, s38\n"
      "s_add_u32 s64, s64, s65\n"
      "s_add_u32 s41, s41, s64\n"
      "s_add_u32 s42, s42, s64\n"
      "s_add_u32 s43, s43, s64\n"
      "s_lshl_b32 s40, s34, 17\n"
      "s_lshl_b32 s64, s99, 13\n"
      "s_add_u32 s40, s40, s64\n"
      "s_lshl_b32 s55, s99, 10\n"
      "s_lshl_b32 s97, s38, 6\n"
      "s_lshl_b32 s77, s38, 5\n"
      "s_lshr_b32 s64, s2, 6\n"
      "s_add_u32 s75, s64, s34\n"
      "s_lshl_b32 s75, s75, 1\n"
      "s_and_b32 s75, s75, 7\n"
      "s_mov_b32 s57, s75\n"
      "s_lshl_b32 s64, s45, 15\n"
      "s_add_u32 s40, s40, s64\n"
      "s_lshl_b32 s48, s45, 16\n"
      "v_mad_u32_u24 v2, v9, s38, v12\n"
      "s_lshl_b32 s64, s99, 2\n"
      "s_and_b32 s64, s64, 4\n"
      "v_lshrrev_b32 v13, 4, v0\n"
      "v_xor_b32 v13, v13, v10\n"
      "v_xor_b32 v13, s64, v13\n"
      "v_lshlrev_b32 v13, 4, v13\n"
      "v_lshl_or_b32 v1, v9, 10, v13\n"
      "s_setprio 1\n"
      "s_waitcnt lgkmcnt(0)\n"
      "s_lshl_b32 s52, s75, 7\n"
      "s_mul_i32 s53, s75, s97\n"
      "s_add_u32 s52, s52, s40\n"
      "s_add_u32 s46, s53, s41\n"
      "s_add_u32 s47, s53, s42\n"
      "s_sub_u32 s49, s52, s48\n"
      "s_add_u32 m0, s55, 0\n"
      "s_add_u32 s71, s52, 0\n"
      "buffer_load_dwordx4 v1, s[4:7], s71 offen lds\n"
      "s_add_u32 m0, s55, 16384\n"
      "s_mov_b32 s71, s46\n"
      "buffer_load_dwordx4 v2, s[24:27], s71 offen lds\n"
      "s_add_u32 m0, s55, 4096\n"
      "s_add_u32 s71, s52, 32768\n"
      "buffer_load_dwordx4 v1, s[4:7], s71 offen lds\n"
      "s_add_u32 m0, s55, 24576\n"
      "s_mov_b32 s71, s47\n"
      "buffer_load_dwordx4 v2, s[24:27], s71 offen lds\n"
      "s_add_u32 m0, s55, 8192\n"
      "s_add_u32 s71, s49, 65536\n"
      "buffer_load_dwordx4 v1, s[4:7], s71 offen lds\n"
      "s_add_u32 m0, s55, 20480\n"
      "s_add_u32 s71, s46, s77\n"
      "buffer_load_dwordx4 v2, s[24:27], s71 offen lds\n"
      "s_add_u32 m0, s55, 12288\n"
      "s_add_u32 s71, s49, 98304\n"
      "buffer_load_dwordx4 v1, s[4:7], s71 offen lds\n"
      "s_add_u32 m0, s55, 28672\n"
      "s_add_u32 s71, s47, s77\n"
      "buffer_load_dwordx4 v2, s[24:27], s71 offen lds\n"
      "v_and_b32 v14, 3, v0\n"
      "v_lshlrev_b32 v13, 4, v14\n"
      "v_mad_u32_u24 v3, v11, s38, v13\n"
      "s_lshl_b32 s64, s99, 4\n"
      "s_mul_i32 s64, s64, s38\n"
      "s_lshl1_add_u32 s44, s73, s64\n"
      "s_add_u32 s72, s72, 128\n"
      "v_mov_b32 v92, 0\n"
      "v_mov_b32 v93, 0\n"
      "s_cmp_lg_u32 s33, 0\n"
      "s_cbranch_scc1 .Lnb_8_%=\n"
      "v_lshlrev_b32 v9, 1, v0\n"
      "s_lshl_b32 s64, s72, 1\n"
      "s_lshl_b32 s65, s73, 1\n"
      "buffer_load_ushort v92, v9, s[28:31], s64 offen\n"
      "buffer_load_ushort v93, v9, s[28:31], s65 offen\n"
      ".Lnb_8_%=:\n"
      "v_lshrrev_b32 v9, 5, v0\n"
      "v_and_b32 v10, 31, v0\n"
      "v_bfe_u32 v11, v0, 2, 2\n"
      "v_bfe_u32 v12, v0, 1, 1\n"
      "v_bfe_u32 v13, v0, 4, 1\n"
      "v_lshl_or_b32 v12, v13, 1, v12\n"
      "v_and_b32 v14, 2, v11\n"
      "v_lshl_or_b32 v12, v14, 1, v12\n"
      "v_and_b32 v14, 1, v0\n"
      "v_lshlrev_b32 v14, 3, v14\n"
      "v_lshl_or_b32 v253, v12, 4, v14\n"
      "v_bfe_u32 v14, v0, 1, 3\n"
      "v_xor_b32 v14, v14, v9\n"
      "v_lshlrev_b32 v14, 4, v14\n"
      "v_lshl_or_b32 v5, v10, 7, v14\n"
      "v_lshl_or_b32 v14, v9, 3, v11\n"
      "v_lshl_or_b32 v6, v14, 7, v253\n"
      "v_lshl_or_b32 v12, v9, 2, v11\n"
      "v_lshl_or_b32 v253, v12, 7, v253\n"
      "s_mov_b32 s60, 0x3fb8aa3b\n"
      "s_mov_b32 s61, 0x3fb8aa3b\n"
      "s_bfe_u32 s63, s2, 0x20003\n"
      "s_bfe_u32 s64, s2, 0x20001\n"
      "s_lshl2_add_u32 s63, s64, s63\n"
      "s_and_b32 s64, s2, 1\n"
      "s_lshl1_add_u32 s36, s63, s64\n"
      "s_lshl2_add_u32 s98, s64, s99\n"
      "s_lshr_b32 s37, s2, 5\n"
      "s_xor_b32 s37, s37, 2\n"
      "s_mul_i32 s96, s99, 12288\n"
      "s_and_b32 s98, s98, 6\n"
      "s_lshl_b32 s64, s37, 7\n"
      "s_mul_i32 s39, s98, 229376\n"
      "s_add_u32 s39, s39, s64\n"
      "s_lshl_b32 s3, s98, 16\n"
      "s_add_u32 s3, s3, s64\n"
      "s_mov_b32 s50, 0\n"
      "s_mov_b32 s51, 0\n"
      "s_lshl3_add_u32 s54, s36, s37\n"
      "s_lshl3_add_u32 s54, s54, s32\n"
      "s_lshl_b32 s54, s54, 7\n"
      "s_add_u32 s54, s54, 15728640\n"
      "v_lshlrev_b32 v252, 3, v0\n"
      "v_cmp_gt_u32 vcc, 32, v0\n"
      "v_mov_b32 v7, v6\n"
      "v_xor_b32 v8, 64, v6\n"
      "v_lshlrev_b32 v15, 6, v14\n"
      "v_lshl_or_b32 v15, v13, 5, v15\n"
      "v_and_b32 v14, 3, v0\n"
      "v_lshl_or_b32 v15, v14, 3, v15\n"
      "v_add_u32 v15, 8192, v15\n"
      "v_lshlrev_b32 v14, 4, v9\n"
      "s_lshl_b32 s64, s57, 7\n"
      "s_add_u32 s64, s64, 147456\n"
      "v_add_u32 v14, s64, v14\n"
      "s_lshl_b32 s64, s99, 12\n"
      "v_add_u32 v9, s64, v5\n"
      "v_xor_b32 v10, 32, v9\n"
      "v_xor_b32 v11, 64, v9\n"
      "v_xor_b32 v12, 0x60, v9\n"
      "v_mov_b64 v[64:65], 0\n"
      "v_mov_b64 v[66:67], 0\n"
      "v_mov_b64 v[68:69], 0\n"
      "v_mov_b64 v[70:71], 0\n"
      "v_mov_b64 v[72:73], 0\n"
      "v_mov_b64 v[74:75], 0\n"
      "v_mov_b64 v[76:77], 0\n"
      "v_mov_b64 v[78:79], 0\n"
      "s_and_b32 s71, s99, 1\n"
      "s_lshl_b32 s28, s71, 18\n"
      "s_add_u32 s28, s28, s39\n"
      "s_add_u32 s28, s28, 0\n"
      "s_lshl_b32 s29, s71, 18\n"
      "s_add_u32 s29, s29, s39\n"
      "s_add_u32 s29, s29, 32768\n"
      "s_lshl_b32 s30, s71, 18\n"
      "s_add_u32 s30, s30, s39\n"
      "s_add_u32 s30, s30, 65536\n"
      "s_lshl_b32 s31, s71, 18\n"
      "s_add_u32 s31, s31, s39\n"
      "s_add_u32 s31, s31, 98304\n"
      "s_lshl_b32 s0, s71, 18\n"
      "s_add_u32 s0, s0, s39\n"
      "s_add_u32 s0, s0, 131072\n"
      "s_lshl_b32 s1, s71, 18\n"
      "s_add_u32 s1, s1, s39\n"
      "s_add_u32 s1, s1, 163840\n"
      "v_add_u32 v96, 7168, v4\n"
      "v_add_u32 v97, 14336, v4\n"
      "v_add_u32 v98, 21504, v4\n"
      "s_lshl2_add_u32 s70, s99, 160784\n"
      "v_mov_b32 v99, s70\n"
      "v_mov_b32 v100, 160784\n"
      "v_mov_b32 v101, 1\n"
      "v_mov_b32 v102, 2\n"
      "v_mov_b32 v103, 3\n"
      "s_add_u32 s75, s75, 1\n"
      "s_and_b32 s75, s75, 7\n"
      "s_lshl_b32 s52, s75, 7\n"
      "s_mul_i32 s53, s75, s97\n"
      "s_add_u32 s52, s52, s40\n"
      "s_add_u32 s46, s53, s41\n"
      "s_add_u32 s47, s53, s42\n"
      "s_sub_u32 s49, s52, s48\n"
      "s_add_u32 m0, s55, 32768\n"
      "s_add_u32 s71, s52, 0\n"
      "buffer_load_dwordx4 v1, s[4:7], s71 offen lds\n"
      "s_add_u32 m0, s55, 49152\n"
      "s_mov_b32 s71, s46\n"
      "buffer_load_dwordx4 v2, s[24:27], s71 offen lds\n"
      "s_add_u32 m0, s55, 36864\n"
      "s_add_u32 s71, s52, 32768\n"
      "buffer_load_dwordx4 v1, s[4:7], s71 offen lds\n"
      "s_add_u32 m0, s55, 57344\n"
      "s_mov_b32 s71, s47\n"
      "buffer_load_dwordx4 v2, s[24:27], s71 offen lds\n"
      "s_add_u32 m0, s55, 40960\n"
      "s_add_u32 s71, s49, 65536\n"
      "buffer_load_dwordx4 v1, s[4:7], s71 offen lds\n"
      "s_add_u32 m0, s55, 53248\n"
      "s_add_u32 s71, s46, s77\n"
      "buffer_load_dwordx4 v2, s[24:27], s71 offen lds\n"
      "s_waitcnt vmcnt(6)\n"
      "s_barrier\n"
      "v_mov_b32 v64, 0x3f80\n"
      "v_cndmask_b32 v68, 0, v92, vcc\n"
      "v_sub_u32 v72, v92, v68\n"
      "v_cndmask_b32 v76, 0, v93, vcc\n"
      "v_mfma_f32_32x32x16_bf16 v[16:31], v[68:71], v[64:67], 0\n"
      "s_add_u32 m0, s55, 45056\n"
      "s_add_u32 s71, s49, 98304\n"
      "buffer_load_dwordx4 v1, s[4:7], s71 offen lds\n"
      "v_mfma_f32_32x32x16_bf16 v[32:47], v[72:75], v[64:67], 0\n"
      "s_add_u32 m0, s55, 61440\n"
      "s_add_u32 s71, s47, s77\n"
      "buffer_load_dwordx4 v2, s[24:27], s71 offen lds\n"
      "v_mfma_f32_32x32x16_bf16 v[48:63], v[76:79], v[64:67], 0\n"
      "s_add_u32 s75, s75, 1\n"
      "s_and_b32 s75, s75, 7\n"
      "s_lshl_b32 s52, s75, 7\n"
      "s_mul_i32 s53, s75, s97\n"
      "s_add_u32 s52, s52, s40\n"
      "s_add_u32 s46, s53, s41\n"
      "s_add_u32 s47, s53, s42\n"
      "s_sub_u32 s49, s52, s48\n"
      "s_add_u32 m0, s55, 65536\n"
      "s_add_u32 s71, s52, 0\n"
      "buffer_load_dwordx4 v1, s[4:7], s71 offen lds\n"
      "ds_read_b128 v[124:127], v9 offset:0\n"
      "ds_read_b128 v[128:131], v10 offset:0\n"
      "ds_read_b128 v[132:135], v11 offset:0\n"
      "ds_read_b128 v[136:139], v12 offset:0\n"
      "s_add_u32 m0, s55, 81920\n"
      "s_mov_b32 s71, s46\n"
      "buffer_load_dwordx4 v2, s[24:27], s71 offen lds\n"
      "s_add_u32 m0, s55, 69632\n"
      "s_add_u32 s71, s52, 32768\n"
      "buffer_load_dwordx4 v1, s[4:7], s71 offen lds\n"
      "s_add_u32 m0, s55, 90112\n"
      "s_mov_b32 s71, s47\n"
      "buffer_load_dwordx4 v2, s[24:27], s71 offen lds\n"
      "s_add_u32 m0, s55, 73728\n"
      "s_add_u32 s71, s49, 65536\n"
      "buffer_load_dwordx4 v1, s[4:7], s71 offen lds\n"
      "s_add_u32 m0, s55, 86016\n"
      "s_add_u32 s71, s46, s77\n"
      "buffer_load_dwordx4 v2, s[24:27], s71 offen lds\n"
      "s_add_u32 m0, s55, 77824\n"
      "s_add_u32 s71, s49, 98304\n"
      "buffer_load_dwordx4 v1, s[4:7], s71 offen lds\n"
      "s_add_u32 m0, s55, 94208\n"
      "s_add_u32 s71, s47, s77\n"
      "buffer_load_dwordx4 v2, s[24:27], s71 offen lds\n"
      "s_add_u32 s75, s75, 1\n"
      "s_and_b32 s75, s75, 7\n"
      "s_lshl_b32 s52, s75, 7\n"
      "s_mul_i32 s53, s75, s97\n"
      "s_add_u32 s52, s52, s40\n"
      "s_add_u32 s46, s53, s41\n"
      "s_add_u32 s47, s53, s42\n"
      "s_sub_u32 s49, s52, s48\n"
      "s_add_u32 m0, s55, 98304\n"
      "s_add_u32 s71, s52, 0\n"
      "buffer_load_dwordx4 v1, s[4:7], s71 offen lds\n"
      "s_add_u32 m0, s55, 114688\n"
      "s_mov_b32 s71, s46\n"
      "buffer_load_dwordx4 v2, s[24:27], s71 offen lds\n"
      "s_add_u32 m0, s55, 102400\n"
      "s_add_u32 s71, s52, 32768\n"
      "buffer_load_dwordx4 v1, s[4:7], s71 offen lds\n"
      "s_add_u32 m0, s55, 122880\n"
      "s_mov_b32 s71, s47\n"
      "buffer_load_dwordx4 v2, s[24:27], s71 offen lds\n"
      "s_add_u32 m0, s55, 106496\n"
      "s_add_u32 s71, s49, 65536\n"
      "buffer_load_dwordx4 v1, s[4:7], s71 offen lds\n"
      "s_add_u32 m0, s55, 118784\n"
      "s_add_u32 s71, s46, s77\n"
      "buffer_load_dwordx4 v2, s[24:27], s71 offen lds\n"
      "s_waitcnt vmcnt(14) lgkmcnt(0)\n"
      "s_barrier\n"
      "s_add_u32 m0, s55, 110592\n"
      "s_add_u32 s71, s49, 98304\n"
      "buffer_load_dwordx4 v1, s[4:7], s71 offen lds\n"
      "ds_read_b128 v[140:143], v9 offset:32768\n"
      "ds_read_b128 v[144:147], v10 offset:32768\n"
      "ds_read_b128 v[148:151], v11 offset:32768\n"
      "ds_read_b128 v[152:155], v12 offset:32768\n"
      "s_add_u32 m0, s55, 126976\n"
      "s_add_u32 s71, s47, s77\n"
      "buffer_load_dwordx4 v2, s[24:27], s71 offen lds\n"
      "s_add_u32 s75, s75, 1\n"
      "s_and_b32 s75, s75, 7\n"
      "s_lshl_b32 s52, s75, 7\n"
      "s_mul_i32 s53, s75, s97\n"
      "s_add_u32 s52, s52, s40\n"
      "s_add_u32 s46, s53, s41\n"
      "s_add_u32 s47, s53, s42\n"
      "s_sub_u32 s49, s52, s48\n"
      "s_add_u32 m0, s55, 0\n"
      "s_add_u32 s71, s52, 0\n"
      "buffer_load_dwordx4 v1, s[4:7], s71 offen lds\n"
      "s_add_u32 m0, s55, 16384\n"
      "s_mov_b32 s71, s46\n"
      "buffer_load_dwordx4 v2, s[24:27], s71 offen lds\n"
      "s_add_u32 m0, s55, 4096\n"
      "s_add_u32 s71, s52, 32768\n"
      "buffer_load_dwordx4 v1, s[4:7], s71 offen lds\n"
      "s_add_u32 m0, s55, 24576\n"
      "s_mov_b32 s71, s47\n"
      "buffer_load_dwordx4 v2, s[24:27], s71 offen lds\n"
      "s_add_u32 m0, s55, 8192\n"
      "s_add_u32 s71, s49, 65536\n"
      "buffer_load_dwordx4 v1, s[4:7], s71 offen lds\n"
      "s_add_u32 m0, s55, 20480\n"
      "s_add_u32 s71, s46, s77\n"
      "buffer_load_dwordx4 v2, s[24:27], s71 offen lds\n"
      "s_waitcnt vmcnt(14) lgkmcnt(0)\n"
      "s_barrier\n"
      "s_add_u32 m0, s55, 12288\n"
      "s_add_u32 s71, s49, 98304\n"
      "buffer_load_dwordx4 v1, s[4:7], s71 offen lds\n"
      "v_xor_b32 v9, 0x10000, v9\n"
      "v_xor_b32 v10, 0x10000, v10\n"
      "v_xor_b32 v11, 0x10000, v11\n"
      "v_xor_b32 v12, 0x10000, v12\n"
      "ds_read_b128 v[156:159], v9 offset:0\n"
      "ds_read_b128 v[160:163], v10 offset:0\n"
      "ds_read_b128 v[164:167], v11 offset:0\n"
      "ds_read_b128 v[168:171], v12 offset:0\n"
      "s_cmp_lg_u32 s99, 2\n"
      "s_cbranch_scc1 .Ltj_9_%=\n"
      "v_lshlrev_b32 v90, 7, v0\n"
      "s_mul_i32 s64, s2, 7168\n"
      "s_lshl_b32 s65, s2, 11\n"
      "s_mov_b32 exec_hi, 0xffffff\n"
      "buffer_load_dword v88, v90, s[8:11], s64 offen\n"
      "s_mov_b64 exec, 0xffff\n"
      "buffer_load_dword v89, v90, s[12:15], s65 offen\n"
      "s_mov_b64 exec, -1\n"
      ".Ltj_9_%=:\n"
      "s_add_u32 m0, s55, 28672\n"
      "s_add_u32 s71, s47, s77\n"
      "buffer_load_dwordx4 v2, s[24:27], s71 offen lds\n"
      "s_add_u32 s75, s75, 1\n"
      "s_and_b32 s75, s75, 7\n"
      "s_lshl_b32 s52, s75, 7\n"
      "s_mul_i32 s53, s75, s97\n"
      "s_add_u32 s52, s52, s40\n"
      "s_add_u32 s46, s53, s41\n"
      "s_add_u32 s47, s53, s42\n"
      "s_sub_u32 s49, s52, s48\n"
      "s_add_u32 m0, s55, 32768\n"
      "s_add_u32 s71, s52, 0\n"
      "buffer_load_dwordx4 v1, s[4:7], s71 offen lds\n"
      "s_add_u32 m0, s55, 49152\n"
      "s_mov_b32 s71, s46\n"
      "buffer_load_dwordx4 v2, s[24:27], s71 offen lds\n"
      "s_add_u32 m0, s55, 36864\n"
      "s_add_u32 s71, s52, 32768\n"
      "buffer_load_dwordx4 v1, s[4:7], s71 offen lds\n"
      "s_add_u32 m0, s55, 57344\n"
      "s_mov_b32 s71, s47\n"
      "buffer_load_dwordx4 v2, s[24:27], s71 offen lds\n"
      "s_add_u32 m0, s55, 40960\n"
      "s_add_u32 s71, s49, 65536\n"
      "buffer_load_dwordx4 v1, s[4:7], s71 offen lds\n"
      "s_add_u32 m0, s55, 53248\n"
      "s_add_u32 s71, s46, s77\n"
      "buffer_load_dwordx4 v2, s[24:27], s71 offen lds\n"
      "s_waitcnt vmcnt(14) lgkmcnt(0)\n"
      "s_barrier\n"
      "s_add_u32 m0, s55, 45056\n"
      "s_add_u32 s71, s49, 98304\n"
      "buffer_load_dwordx4 v1, s[4:7], s71 offen lds\n"
      "ds_read_b128 v[172:175], v9 offset:32768\n"
      "ds_read_b128 v[176:179], v10 offset:32768\n"
      "ds_read_b128 v[180:183], v11 offset:32768\n"
      "ds_read_b128 v[184:187], v12 offset:32768\n"
      "s_add_u32 m0, s55, 61440\n"
      "s_add_u32 s71, s47, s77\n"
      "buffer_load_dwordx4 v2, s[24:27], s71 offen lds\n"
      "s_add_u32 s75, s75, 1\n"
      "s_and_b32 s75, s75, 7\n"
      "s_lshl_b32 s52, s75, 7\n"
      "s_mul_i32 s53, s75, s97\n"
      "s_add_u32 s52, s52, s40\n"
      "s_add_u32 s46, s53, s41\n"
      "s_add_u32 s47, s53, s42\n"
      "s_sub_u32 s49, s52, s48\n"
      "s_add_u32 m0, s55, 65536\n"
      "s_add_u32 s71, s52, 0\n"
      "buffer_load_dwordx4 v1, s[4:7], s71 offen lds\n"
      "s_add_u32 m0, s55, 81920\n"
      "s_mov_b32 s71, s46\n"
      "buffer_load_dwordx4 v2, s[24:27], s71 offen lds\n"
      "s_add_u32 m0, s55, 69632\n"
      "s_add_u32 s71, s52, 32768\n"
      "buffer_load_dwordx4 v1, s[4:7], s71 offen lds\n"
      "s_add_u32 m0, s55, 90112\n"
      "s_mov_b32 s71, s47\n"
      "buffer_load_dwordx4 v2, s[24:27], s71 offen lds\n"
      "s_add_u32 m0, s55, 73728\n"
      "s_add_u32 s71, s49, 65536\n"
      "buffer_load_dwordx4 v1, s[4:7], s71 offen lds\n"
      "s_add_u32 m0, s55, 86016\n"
      "s_add_u32 s71, s46, s77\n"
      "buffer_load_dwordx4 v2, s[24:27], s71 offen lds\n"
      "s_waitcnt vmcnt(14) lgkmcnt(0)\n"
      "s_barrier\n"
      "s_add_u32 m0, s55, 77824\n"
      "s_add_u32 s71, s49, 98304\n"
      "buffer_load_dwordx4 v1, s[4:7], s71 offen lds\n"
      "v_xor_b32 v9, 0x10000, v9\n"
      "v_xor_b32 v10, 0x10000, v10\n"
      "v_xor_b32 v11, 0x10000, v11\n"
      "v_xor_b32 v12, 0x10000, v12\n"
      "ds_read_b128 v[188:191], v9 offset:0\n"
      "ds_read_b128 v[192:195], v10 offset:0\n"
      "ds_read_b128 v[196:199], v11 offset:0\n"
      "ds_read_b128 v[200:203], v12 offset:0\n"
      "s_add_u32 m0, s55, 94208\n"
      "s_add_u32 s71, s47, s77\n"
      "buffer_load_dwordx4 v2, s[24:27], s71 offen lds\n"
      "s_add_u32 s75, s75, 1\n"
      "s_and_b32 s75, s75, 7\n"
      "s_lshl_b32 s52, s75, 7\n"
      "s_mul_i32 s53, s75, s97\n"
      "s_add_u32 s52, s52, s40\n"
      "s_add_u32 s46, s53, s41\n"
      "s_add_u32 s47, s53, s42\n"
      "s_sub_u32 s49, s52, s48\n"
      "s_add_u32 m0, s55, 98304\n"
      "s_add_u32 s71, s52, 0\n"
      "buffer_load_dwordx4 v1, s[4:7], s71 offen lds\n"
      "s_add_u32 m0, s55, 114688\n"
      "s_mov_b32 s71, s46\n"
      "buffer_load_dwordx4 v2, s[24:27], s71 offen lds\n"
      "s_add_u32 m0, s55, 102400\n"
      "s_add_u32 s71, s52, 32768\n"
      "buffer_load_dwordx4 v1, s[4:7], s71 offen lds\n"
      "s_add_u32 m0, s55, 122880\n"
      "s_mov_b32 s71, s47\n"
      "buffer_load_dwordx4 v2, s[24:27], s71 offen lds\n"
      "s_add_u32 m0, s55, 106496\n"
      "s_add_u32 s71, s49, 65536\n"
      "buffer_load_dwordx4 v1, s[4:7], s71 offen lds\n"
      "s_add_u32 m0, s55, 118784\n"
      "s_add_u32 s71, s46, s77\n"
      "buffer_load_dwordx4 v2, s[24:27], s71 offen lds\n"
      "s_waitcnt vmcnt(14) lgkmcnt(0)\n"
      "s_barrier\n"
      "s_add_u32 m0, s55, 110592\n"
      "s_add_u32 s71, s49, 98304\n"
      "buffer_load_dwordx4 v1, s[4:7], s71 offen lds\n"
      "ds_read_b128 v[204:207], v9 offset:32768\n"
      "ds_read_b128 v[208:211], v10 offset:32768\n"
      "ds_read_b128 v[212:215], v11 offset:32768\n"
      "ds_read_b128 v[216:219], v12 offset:32768\n"
      "s_cmp_lg_u32 s99, 1\n"
      "s_cbranch_scc1 .Ltj_10_%=\n"
      "v_lshlrev_b32 v90, 10, v0\n"
      "s_lshr_b32 s64, s2, 3\n"
      "s_and_b32 s65, s2, 1\n"
      "s_lshl_b32 s65, s65, 5\n"
      "s_add_u32 s64, s64, s65\n"
      "s_mul_i32 s65, s64, 28672\n"
      "s_lshl_b32 s64, s64, 13\n"
      "s_mov_b64 exec, 0xfffffff\n"
      "buffer_load_dword v88, v90, s[8:11], s65 offen offset:384\n"
      "s_mov_b64 exec, 0xff\n"
      "buffer_load_dword v89, v90, s[12:15], s64 offen offset:384\n"
      "s_mov_b64 exec, -1\n"
      ".Ltj_10_%=:\n"
      "s_add_u32 m0, s55, 126976\n"
      "s_add_u32 s71, s47, s77\n"
      "buffer_load_dwordx4 v2, s[24:27], s71 offen lds\n"
      "s_add_u32 s75, s75, 1\n"
      "s_and_b32 s75, s75, 7\n"
      "s_mul_i32 s53, s75, s97\n"
      "s_add_u32 m0, s55, 0\n"
      "s_add_u32 s52, s53, s43\n"
      "buffer_load_dwordx4 v2, s[24:27], s52 offen lds\n"
      "s_add_u32 m0, s55, 4096\n"
      "s_add_u32 s71, s52, s77\n"
      "buffer_load_dwordx4 v2, s[24:27], s71 offen lds\n"
      "s_cmp_lg_u32 s33, 0\n"
      "s_cbranch_scc1 .Lnh_11_%=\n"
      "s_add_u32 m0, s55, 8192\n"
      "s_add_u32 s71, s53, s44\n"
      "buffer_load_dwordx4 v3, s[24:27], s71 offen lds\n"
      ".Lnh_11_%=:\n"
      "ds_read_b32 v94, v100 offset:16\n"
      "s_waitcnt vmcnt(11) lgkmcnt(0)\n"
      "s_cmp_eq_u32 s33, 0\n"
      "s_cbranch_scc1 .Lw2_12_%=\n"
      "s_waitcnt vmcnt(10)\n"
      ".Lw2_12_%=:\n"
      "s_barrier\n"
      "v_xor_b32 v9, 0x10000, v9\n"
      "v_xor_b32 v10, 0x10000, v10\n"
      "v_xor_b32 v11, 0x10000, v11\n"
      "v_xor_b32 v12, 0x10000, v12\n"
      "ds_read_b128 v[220:223], v9 offset:0\n"
      "ds_read_b128 v[224:227], v10 offset:0\n"
      "ds_read_b128 v[228:231], v11 offset:0\n"
      "ds_read_b128 v[232:235], v12 offset:0\n"
      "s_add_u32 s75, s75, 1\n"
      "s_and_b32 s75, s75, 7\n"
      "s_mul_i32 s53, s75, s97\n"
      "s_add_u32 m0, s55, 12288\n"
      "s_add_u32 s52, s53, s43\n"
      "buffer_load_dwordx4 v2, s[24:27], s52 offen lds\n"
      "s_add_u32 m0, s55, 16384\n"
      "s_add_u32 s71, s52, s77\n"
      "buffer_load_dwordx4 v2, s[24:27], s71 offen lds\n"
      "s_cmp_lg_u32 s33, 0\n"
      "s_cbranch_scc1 .Lnh_13_%=\n"
      "s_add_u32 m0, s55, 20480\n"
      "s_add_u32 s71, s53, s44\n"
      "buffer_load_dwordx4 v3, s[24:27], s71 offen lds\n"
      ".Lnh_13_%=:\n"
      "s_add_u32 s75, s75, 1\n"
      "s_and_b32 s75, s75, 7\n"
      "s_mul_i32 s53, s75, s97\n"
      "s_add_u32 m0, s55, 24576\n"
      "s_add_u32 s52, s53, s43\n"
      "buffer_load_dwordx4 v2, s[24:27], s52 offen lds\n"
      "s_add_u32 m0, s55, 28672\n"
      "s_add_u32 s71, s52, s77\n"
      "buffer_load_dwordx4 v2, s[24:27], s71 offen lds\n"
      "s_cmp_lg_u32 s33, 0\n"
      "s_cbranch_scc1 .Lnh_14_%=\n"
      "s_add_u32 m0, s55, 32768\n"
      "s_add_u32 s71, s53, s44\n"
      "buffer_load_dwordx4 v3, s[24:27], s71 offen lds\n"
      ".Lnh_14_%=:\n"
      "s_add_u32 s75, s75, 1\n"
      "s_and_b32 s75, s75, 7\n"
      "s_mul_i32 s53, s75, s97\n"
      "s_add_u32 m0, s55, 36864\n"
      "s_add_u32 s52, s53, s43\n"
      "buffer_load_dwordx4 v2, s[24:27], s52 offen lds\n"
      "s_add_u32 m0, s55, 40960\n"
      "s_add_u32 s71, s52, s77\n"
      "buffer_load_dwordx4 v2, s[24:27], s71 offen lds\n"
      "s_cmp_lg_u32 s33, 0\n"
      "s_cbranch_scc1 .Lnh_15_%=\n"
      "s_add_u32 m0, s55, 45056\n"
      "s_add_u32 s71, s53, s44\n"
      "buffer_load_dwordx4 v3, s[24:27], s71 offen lds\n"
      ".Lnh_15_%=:\n"
      "v_readfirstlane_b32 s59, v94\n"
      "s_nop 0\n"
      "s_add_u32 s59, s59, 1\n"
      "s_not_b32 s58, s59\n"
      "s_and_b32 s65, s99, 1\n"
      "s_xor_b32 s62, s36, 1\n"
      "s_lshl1_add_u32 s62, s62, s65\n"
      "s_mul_i32 s62, s62, 147456\n"
      "s_mul_i32 s63, s98, 14336\n"
      "s_add_u32 s63, s63, s62\n"
      "s_add_u32 s46, s63, 1048576\n"
      "s_xor_b32 s62, s36, 0\n"
      "s_lshl1_add_u32 s62, s62, s65\n"
      "s_mul_i32 s62, s62, 147456\n"
      "s_mul_i32 s63, s98, 14336\n"
      "s_add_u32 s63, s63, s62\n"
      "s_add_u32 s48, s63, 1048576\n"
      "s_xor_b32 s63, s98, 0\n"
      "s_lshl_b32 s63, s63, 12\n"
      "s_add_u32 s63, s63, s62\n"
      "s_add_u32 s49, s63, 1163264\n"
      "s_xor_b32 s63, s98, 4\n"
      "s_lshl_b32 s63, s63, 12\n"
      "s_add_u32 s63, s63, s62\n"
      "s_add_u32 s47, s63, 1163264\n"
      "s_waitcnt vmcnt(12) lgkmcnt(0)\n"
      "s_cmp_eq_u32 s33, 0\n"
      "s_cbranch_scc1 .Lw2_16_%=\n"
      "s_waitcnt vmcnt(8)\n"
      ".Lw2_16_%=:\n"
      "s_barrier\n"
      "ds_read_b128 v[236:239], v9 offset:32768\n"
      "ds_read_b128 v[240:243], v10 offset:32768\n"
      "ds_read_b128 v[244:247], v11 offset:32768\n"
      "ds_read_b128 v[248:251], v12 offset:32768\n"
      "s_add_u32 s75, s75, 1\n"
      "s_and_b32 s75, s75, 7\n"
      "s_mul_i32 s53, s75, s97\n"
      "s_add_u32 m0, s55, 49152\n"
      "s_add_u32 s52, s53, s43\n"
      "buffer_load_dwordx4 v2, s[24:27], s52 offen lds\n"
      "s_add_u32 m0, s55, 53248\n"
      "s_add_u32 s71, s52, s77\n"
      "buffer_load_dwordx4 v2, s[24:27], s71 offen lds\n"
      "s_cmp_lg_u32 s33, 0\n"
      "s_cbranch_scc1 .Lnh_17_%=\n"
      "s_add_u32 m0, s55, 57344\n"
      "s_add_u32 s71, s53, s44\n"
      "buffer_load_dwordx4 v3, s[24:27], s71 offen lds\n"
      ".Lnh_17_%=:\n"
      "s_add_u32 s75, s75, 1\n"
      "s_and_b32 s75, s75, 7\n"
      "s_mul_i32 s53, s75, s97\n"
      "s_add_u32 m0, s55, 61440\n"
      "s_add_u32 s52, s53, s43\n"
      "buffer_load_dwordx4 v2, s[24:27], s52 offen lds\n"
      "s_add_u32 m0, s55, 65536\n"
      "s_add_u32 s71, s52, s77\n"
      "buffer_load_dwordx4 v2, s[24:27], s71 offen lds\n"
      "s_cmp_lg_u32 s33, 0\n"
      "s_cbranch_scc1 .Lnh_18_%=\n"
      "s_add_u32 m0, s55, 69632\n"
      "s_add_u32 s71, s53, s44\n"
      "buffer_load_dwordx4 v3, s[24:27], s71 offen lds\n"
      ".Lnh_18_%=:\n"
      "s_add_u32 s75, s75, 1\n"
      "s_and_b32 s75, s75, 7\n"
      "s_mul_i32 s53, s75, s97\n"
      "s_add_u32 m0, s55, 73728\n"
      "s_add_u32 s52, s53, s43\n"
      "buffer_load_dwordx4 v2, s[24:27], s52 offen lds\n"
      "s_add_u32 m0, s55, 77824\n"
      "s_add_u32 s71, s52, s77\n"
      "buffer_load_dwordx4 v2, s[24:27], s71 offen lds\n"
      "s_cmp_lg_u32 s33, 0\n"
      "s_cbranch_scc1 .Lnh_19_%=\n"
      "s_add_u32 m0, s55, 81920\n"
      "s_add_u32 s71, s53, s44\n"
      "buffer_load_dwordx4 v3, s[24:27], s71 offen lds\n"
      ".Lnh_19_%=:\n"
      "s_xor_b32 s63, s99, s45\n"
      "s_lshl2_add_u32 s63, s34, s63\n"
      "s_mul_i32 s63, s63, 147456\n"
      "s_add_u32 s63, s63, 1048576\n"
      "s_cmp_lg_u32 s33, 0\n"
      "s_cbranch_scc1 .Lbq_20_%=\n"
      "s_lshl_b32 s62, s72, 6\n"
      "s_add_u32 s40, s63, s62\n"
      "s_add_u32 s41, s40, 2048\n"
      "s_lshl_b32 s62, s73, 6\n"
      "s_add_u32 s42, s63, s62\n"
      "s_branch .Lbx_21_%=\n"
      ".Lbq_20_%=:\n"
      "s_lshl_b32 s62, s35, 12\n"
      "s_add_u32 s63, s63, s62\n"
      "s_add_u32 s40, s63, 114688\n"
      "s_xor_b32 s63, s99, s45\n"
      "s_lshl3_add_u32 s62, s34, s35\n"
      "s_lshl2_add_u32 s62, s62, s63\n"
      "s_lshl_b32 s62, s62, 7\n"
      "s_add_u32 s42, s62, 11042816\n"
      ".Lbx_21_%=:\n"
      "s_lshr_b32 s62, s35, 2\n"
      "s_lshl1_add_u32 s62, s34, s62\n"
      "s_lshl_b32 s45, s62, 13\n"
      "s_and_b32 s62, s35, 2\n"
      "s_lshl_b32 s62, s62, 7\n"
      "s_add_u32 s45, s45, s62\n"
      "s_and_b32 s62, s35, 1\n"
      "s_lshl4_add_u32 s45, s62, s45\n"
      "s_and_b32 s62, s99, 1\n"
      "s_lshl_b32 s62, s62, 7\n"
      "s_add_u32 s45, s45, s62\n"
      "s_lshr_b32 s62, s99, 1\n"
      "s_lshl_b32 s62, s62, 3\n"
      "s_add_u32 s45, s45, s62\n"
      "s_add_u32 s45, s45, 15729152\n"
      "s_cmp_eq_u32 s33, 0\n"
      "s_cbranch_scc1 .Lby_22_%=\n"
      "s_add_u32 s45, s45, 262144\n"
      "s_cmp_ge_u32 s99, 2\n"
      "s_cbranch_scc1 .Lby_22_%=\n"
      "s_xor_b32 s45, s45, 0x2000\n"
      ".Lby_22_%=:\n"
      "s_waitcnt vmcnt(9) lgkmcnt(0)\n"
      "s_cmp_eq_u32 s33, 0\n"
      "s_cbranch_scc1 .Lw2_23_%=\n"
      "s_waitcnt vmcnt(6)\n"
      ".Lw2_23_%=:\n"
      "s_barrier\n"
      "s_cmp_lg_u32 s33, 0\n"
      "s_cbranch_scc1 .LQB_7_%=\n"
      "s_setprio 3\n"
      "ds_read_b64_tr_b16 v[64:65], v7 offset:0\n"
      "ds_read_b64_tr_b16 v[66:67], v7 offset:512\n"
      "ds_read_b64_tr_b16 v[68:69], v8 offset:0\n"
      "ds_read_b64_tr_b16 v[70:71], v8 offset:512\n"
      "ds_read_b64_tr_b16 v[72:73], v15 offset:0\n"
      "ds_read_b64_tr_b16 v[74:75], v15 offset:256\n"
      "ds_read_b64_tr_b16 v[104:105], v7 offset:2048\n"
      "ds_read_b64_tr_b16 v[106:107], v7 offset:2560\n"
      "ds_read_b64_tr_b16 v[108:109], v8 offset:2048\n"
      "ds_read_b64_tr_b16 v[110:111], v8 offset:2560\n"
      "ds_read_b64_tr_b16 v[112:113], v15 offset:1024\n"
      "ds_read_b64_tr_b16 v[114:115], v15 offset:1280\n"
      "s_waitcnt lgkmcnt(6)\n"
      "v_mfma_f32_32x32x16_bf16 v[16:31], v[64:67], v[124:127], v[16:31]\n"
      "ds_read_b64_tr_b16 v[76:77], v7 offset:4096\n"
      "ds_read_b64_tr_b16 v[78:79], v7 offset:4608\n"
      "v_mfma_f32_32x32x16_bf16 v[32:47], v[68:71], v[124:127], v[32:47]\n"
      "ds_read_b64_tr_b16 v[116:117], v8 offset:4096\n"
      "ds_read_b64_tr_b16 v[118:119], v8 offset:4608\n"
      "s_add_u32 s75, s75, 1\n"
      "s_and_b32 s75, s75, 7\n"
      "s_mul_i32 s53, s75, s97\n"
      "s_add_u32 m0, s55, 86016\n"
      "s_add_u32 s52, s53, s43\n"
      "buffer_load_dwordx4 v2, s[24:27], s52 offen lds\n"
      "v_mfma_f32_32x32x16_bf16 v[48:63], v[72:75], v[124:127], v[48:63]\n"
      "ds_read_b64_tr_b16 v[120:121], v15 offset:2048\n"
      "ds_read_b64_tr_b16 v[122:123], v15 offset:2304\n"
      "s_waitcnt lgkmcnt(6)\n"
      "v_mfma_f32_32x32x16_bf16 v[16:31], v[104:107], v[128:131], v[16:31]\n"
      "ds_read_b64_tr_b16 v[64:65], v7 offset:6144\n"
      "ds_read_b64_tr_b16 v[66:67], v7 offset:6656\n"
      "v_mfma_f32_32x32x16_bf16 v[32:47], v[108:111], v[128:131], v[32:47]\n"
      "ds_read_b64_tr_b16 v[68:69], v8 offset:6144\n"
      "ds_read_b64_tr_b16 v[70:71], v8 offset:6656\n"
      "s_add_u32 m0, s55, 90112\n"
      "s_add_u32 s71, s52, s77\n"
      "buffer_load_dwordx4 v2, s[24:27], s71 offen lds\n"
      "v_mfma_f32_32x32x16_bf16 v[48:63], v[112:115], v[128:131], v[48:63]\n"
      "ds_read_b64_tr_b16 v[72:73], v15 offset:3072\n"
      "ds_read_b64_tr_b16 v[74:75], v15 offset:3328\n"
      "s_waitcnt lgkmcnt(6)\n"
      "v_mfma_f32_32x32x16_bf16 v[16:31], v[76:79], v[132:135], v[16:31]\n"
      "ds_read_b64_tr_b16 v[104:105], v7 offset:12288\n"
      "ds_read_b64_tr_b16 v[106:107], v7 offset:12800\n"
      "v_mfma_f32_32x32x16_bf16 v[32:47], v[116:119], v[132:135], v[32:47]\n"
      "ds_read_b64_tr_b16 v[108:109], v8 offset:12288\n"
      "ds_read_b64_tr_b16 v[110:111], v8 offset:12800\n"
      "s_add_u32 m0, s55, 94208\n"
      "s_add_u32 s71, s53, s44\n"
      "buffer_load_dwordx4 v3, s[24:27], s71 offen lds\n"
      "v_mfma_f32_32x32x16_bf16 v[48:63], v[120:123], v[132:135], v[48:63]\n"
      "ds_read_b64_tr_b16 v[112:113], v15 offset:12288\n"
      "ds_read_b64_tr_b16 v[114:115], v15 offset:12544\n"
      "s_waitcnt lgkmcnt(6)\n"
      "v_mfma_f32_32x32x16_bf16 v[16:31], v[64:67], v[136:139], v[16:31]\n"
      "ds_read_b64_tr_b16 v[76:77], v7 offset:14336\n"
      "ds_read_b64_tr_b16 v[78:79], v7 offset:14848\n"
      "v_mfma_f32_32x32x16_bf16 v[32:47], v[68:71], v[136:139], v[32:47]\n"
      "ds_read_b64_tr_b16 v[116:117], v8 offset:14336\n"
      "ds_read_b64_tr_b16 v[118:119], v8 offset:14848\n"
      "s_add_u32 m0, s96, 98304\n"
      "s_nop 0\n"
      "buffer_load_dwordx4 v4, s[8:11], s28 offen offset:0 lds\n"
      "v_mfma_f32_32x32x16_bf16 v[48:63], v[72:75], v[136:139], v[48:63]\n"
      "ds_read_b64_tr_b16 v[120:121], v15 offset:13312\n"
      "ds_read_b64_tr_b16 v[122:123], v15 offset:13568\n"
      "s_waitcnt lgkmcnt(6)\n"
      "v_mfma_f32_32x32x16_bf16 v[16:31], v[104:107], v[140:143], v[16:31]\n"
      "ds_read_b64_tr_b16 v[64:65], v7 offset:16384\n"
      "ds_read_b64_tr_b16 v[66:67], v7 offset:16896\n"
      "v_mfma_f32_32x32x16_bf16 v[32:47], v[108:111], v[140:143], v[32:47]\n"
      "ds_read_b64_tr_b16 v[68:69], v8 offset:16384\n"
      "ds_read_b64_tr_b16 v[70:71], v8 offset:16896\n"
      "buffer_load_dwordx4 v96, s[8:11], s28 offen offset:1024 lds\n"
      "v_mfma_f32_32x32x16_bf16 v[48:63], v[112:115], v[140:143], v[48:63]\n"
      "ds_read_b64_tr_b16 v[72:73], v15 offset:14336\n"
      "ds_read_b64_tr_b16 v[74:75], v15 offset:14592\n"
      "s_waitcnt lgkmcnt(6)\n"
      "v_mfma_f32_32x32x16_bf16 v[16:31], v[76:79], v[144:147], v[16:31]\n"
      "ds_read_b64_tr_b16 v[104:105], v7 offset:18432\n"
      "ds_read_b64_tr_b16 v[106:107], v7 offset:18944\n"
      "v_mfma_f32_32x32x16_bf16 v[32:47], v[116:119], v[144:147], v[32:47]\n"
      "ds_read_b64_tr_b16 v[108:109], v8 offset:18432\n"
      "ds_read_b64_tr_b16 v[110:111], v8 offset:18944\n"
      "buffer_load_dwordx4 v97, s[8:11], s28 offen offset:2048 lds\n"
      "v_mfma_f32_32x32x16_bf16 v[48:63], v[120:123], v[144:147], v[48:63]\n"
      "ds_read_b64_tr_b16 v[112:113], v15 offset:15360\n"
      "ds_read_b64_tr_b16 v[114:115], v15 offset:15616\n"
      "s_waitcnt lgkmcnt(6)\n"
      "v_mfma_f32_32x32x16_bf16 v[16:31], v[64:67], v[148:151], v[16:31]\n"
      "ds_read_b64_tr_b16 v[76:77], v7 offset:24576\n"
      "ds_read_b64_tr_b16 v[78:79], v7 offset:25088\n"
      "v_mfma_f32_32x32x16_bf16 v[32:47], v[68:71], v[148:151], v[32:47]\n"
      "ds_read_b64_tr_b16 v[116:117], v8 offset:24576\n"
      "ds_read_b64_tr_b16 v[118:119], v8 offset:25088\n"
      "buffer_load_dwordx4 v98, s[8:11], s28 offen offset:3072 lds\n"
      "v_mfma_f32_32x32x16_bf16 v[48:63], v[72:75], v[148:151], v[48:63]\n"
      "ds_read_b64_tr_b16 v[120:121], v15 offset:24576\n"
      "ds_read_b64_tr_b16 v[122:123], v15 offset:24832\n"
      "s_waitcnt lgkmcnt(6)\n"
      "v_mfma_f32_32x32x16_bf16 v[16:31], v[104:107], v[152:155], v[16:31]\n"
      "ds_read_b64_tr_b16 v[64:65], v7 offset:26624\n"
      "ds_read_b64_tr_b16 v[66:67], v7 offset:27136\n"
      "v_mfma_f32_32x32x16_bf16 v[32:47], v[108:111], v[152:155], v[32:47]\n"
      "ds_read_b64_tr_b16 v[68:69], v8 offset:26624\n"
      "ds_read_b64_tr_b16 v[70:71], v8 offset:27136\n"
      "s_add_u32 m0, s96, 102400\n"
      "s_nop 0\n"
      "buffer_load_dwordx4 v4, s[8:11], s29 offen offset:0 lds\n"
      "v_mfma_f32_32x32x16_bf16 v[48:63], v[112:115], v[152:155], v[48:63]\n"
      "ds_read_b64_tr_b16 v[72:73], v15 offset:25600\n"
      "ds_read_b64_tr_b16 v[74:75], v15 offset:25856\n"
      "s_waitcnt lgkmcnt(6)\n"
      "v_mfma_f32_32x32x16_bf16 v[16:31], v[76:79], v[156:159], v[16:31]\n"
      "s_waitcnt vmcnt(5)\n"
      "ds_write_b32 v99, v101\n"
      "ds_read_b64_tr_b16 v[104:105], v7 offset:28672\n"
      "ds_read_b64_tr_b16 v[106:107], v7 offset:29184\n"
      "v_mfma_f32_32x32x16_bf16 v[32:47], v[116:119], v[156:159], v[32:47]\n"
      "ds_read_b64_tr_b16 v[108:109], v8 offset:28672\n"
      "ds_read_b64_tr_b16 v[110:111], v8 offset:29184\n"
      "buffer_load_dwordx4 v96, s[8:11], s29 offen offset:1024 lds\n"
      "v_mfma_f32_32x32x16_bf16 v[48:63], v[120:123], v[156:159], v[48:63]\n"
      "ds_read_b64_tr_b16 v[112:113], v15 offset:26624\n"
      "ds_read_b64_tr_b16 v[114:115], v15 offset:26880\n"
      "s_waitcnt lgkmcnt(6)\n"
      "v_mfma_f32_32x32x16_bf16 v[16:31], v[64:67], v[160:163], v[16:31]\n"
      "ds_read_b64_tr_b16 v[76:77], v7 offset:30720\n"
      "ds_read_b64_tr_b16 v[78:79], v7 offset:31232\n"
      "v_mfma_f32_32x32x16_bf16 v[32:47], v[68:71], v[160:163], v[32:47]\n"
      "ds_read_b64_tr_b16 v[116:117], v8 offset:30720\n"
      "ds_read_b64_tr_b16 v[118:119], v8 offset:31232\n"
      "buffer_load_dwordx4 v97, s[8:11], s29 offen offset:2048 lds\n"
      "v_mfma_f32_32x32x16_bf16 v[48:63], v[72:75], v[160:163], v[48:63]\n"
      "ds_read_b64_tr_b16 v[120:121], v15 offset:27648\n"
      "ds_read_b64_tr_b16 v[122:123], v15 offset:27904\n"
      "s_waitcnt lgkmcnt(6)\n"
      "v_mfma_f32_32x32x16_bf16 v[16:31], v[104:107], v[164:167], v[16:31]\n"
      "ds_read_b64_tr_b16 v[64:65], v7 offset:36864\n"
      "ds_read_b64_tr_b16 v[66:67], v7 offset:37376\n"
      "v_mfma_f32_32x32x16_bf16 v[32:47], v[108:111], v[164:167], v[32:47]\n"
      "ds_read_b64_tr_b16 v[68:69], v8 offset:36864\n"
      "ds_read_b64_tr_b16 v[70:71], v8 offset:37376\n"
      "buffer_load_dwordx4 v98, s[8:11], s29 offen offset:3072 lds\n"
      "v_mfma_f32_32x32x16_bf16 v[48:63], v[112:115], v[164:167], v[48:63]\n"
      "ds_read_b64_tr_b16 v[72:73], v15 offset:36864\n"
      "ds_read_b64_tr_b16 v[74:75], v15 offset:37120\n"
      "s_waitcnt lgkmcnt(6)\n"
      "v_mfma_f32_32x32x16_bf16 v[16:31], v[76:79], v[168:171], v[16:31]\n"
      "ds_read_b64_tr_b16 v[104:105], v7 offset:38912\n"
      "ds_read_b64_tr_b16 v[106:107], v7 offset:39424\n"
      "v_mfma_f32_32x32x16_bf16 v[32:47], v[116:119], v[168:171], v[32:47]\n"
      "ds_read_b64_tr_b16 v[108:109], v8 offset:38912\n"
      "ds_read_b64_tr_b16 v[110:111], v8 offset:39424\n"
      "s_add_u32 m0, s96, 106496\n"
      "s_nop 0\n"
      "buffer_load_dwordx4 v4, s[8:11], s30 offen offset:0 lds\n"
      "v_mfma_f32_32x32x16_bf16 v[48:63], v[120:123], v[168:171], v[48:63]\n"
      "ds_read_b64_tr_b16 v[112:113], v15 offset:37888\n"
      "ds_read_b64_tr_b16 v[114:115], v15 offset:38144\n"
      "s_waitcnt lgkmcnt(6)\n"
      "v_mfma_f32_32x32x16_bf16 v[16:31], v[64:67], v[172:175], v[16:31]\n"
      "ds_read_b128 v[88:91], v100\n"
      "ds_read_b64_tr_b16 v[76:77], v7 offset:40960\n"
      "ds_read_b64_tr_b16 v[78:79], v7 offset:41472\n"
      "v_mfma_f32_32x32x16_bf16 v[32:47], v[68:71], v[172:175], v[32:47]\n"
      "ds_read_b64_tr_b16 v[116:117], v8 offset:40960\n"
      "ds_read_b64_tr_b16 v[118:119], v8 offset:41472\n"
      "buffer_load_dwordx4 v96, s[8:11], s30 offen offset:1024 lds\n"
      "v_mfma_f32_32x32x16_bf16 v[48:63], v[72:75], v[172:175], v[48:63]\n"
      "ds_read_b64_tr_b16 v[120:121], v15 offset:38912\n"
      "ds_read_b64_tr_b16 v[122:123], v15 offset:39168\n"
      "s_waitcnt lgkmcnt(6)\n"
      "v_mfma_f32_32x32x16_bf16 v[16:31], v[104:107], v[176:179], v[16:31]\n"
      "v_min3_u32 v88, v88, v89, v90\n"
      "v_min_u32 v88, v88, v91\n"
      "v_cmp_le_u32 vcc, 1, v88\n"
      "s_cbranch_vccz .Llz_24_%=\n"
      ".Llzr_25_%=:\n"
      "ds_read_b64_tr_b16 v[64:65], v7 offset:43008\n"
      "ds_read_b64_tr_b16 v[66:67], v7 offset:43520\n"
      "v_mfma_f32_32x32x16_bf16 v[32:47], v[108:111], v[176:179], v[32:47]\n"
      "ds_read_b64_tr_b16 v[68:69], v8 offset:43008\n"
      "ds_read_b64_tr_b16 v[70:71], v8 offset:43520\n"
      "buffer_load_dwordx4 v97, s[8:11], s30 offen offset:2048 lds\n"
      "v_mfma_f32_32x32x16_bf16 v[48:63], v[112:115], v[176:179], v[48:63]\n"
      "ds_read_b64_tr_b16 v[72:73], v15 offset:39936\n"
      "ds_read_b64_tr_b16 v[74:75], v15 offset:40192\n"
      "buffer_load_dwordx4 v98, s[8:11], s30 offen offset:3072 lds\n"
      "s_waitcnt lgkmcnt(6)\n"
      "v_mfma_f32_32x32x16_bf16 v[16:31], v[76:79], v[180:183], v[16:31]\n"
      "v_add_u32 v7, 49152, v7\n"
      "v_add_u32 v8, 49152, v8\n"
      "v_add_u32 v15, 49152, v15\n"
      "ds_read_b64_tr_b16 v[104:105], v7 offset:0\n"
      "ds_read_b64_tr_b16 v[106:107], v7 offset:512\n"
      "v_mfma_f32_32x32x16_bf16 v[32:47], v[116:119], v[180:183], v[32:47]\n"
      "ds_read_b64_tr_b16 v[108:109], v8 offset:0\n"
      "ds_read_b64_tr_b16 v[110:111], v8 offset:512\n"
      "v_mfma_f32_32x32x16_bf16 v[48:63], v[120:123], v[180:183], v[48:63]\n"
      "ds_read_b64_tr_b16 v[112:113], v15 offset:0\n"
      "ds_read_b64_tr_b16 v[114:115], v15 offset:256\n"
      "s_waitcnt lgkmcnt(6)\n"
      "v_mfma_f32_32x32x16_bf16 v[16:31], v[64:67], v[184:187], v[16:31]\n"
      "ds_write_b32 v99, v102\n"
      "ds_read_b64_tr_b16 v[76:77], v7 offset:2048\n"
      "ds_read_b64_tr_b16 v[78:79], v7 offset:2560\n"
      "v_mfma_f32_32x32x16_bf16 v[32:47], v[68:71], v[184:187], v[32:47]\n"
      "ds_read_b64_tr_b16 v[116:117], v8 offset:2048\n"
      "ds_read_b64_tr_b16 v[118:119], v8 offset:2560\n"
      "v_mfma_f32_32x32x16_bf16 v[48:63], v[72:75], v[184:187], v[48:63]\n"
      "ds_read_b64_tr_b16 v[120:121], v15 offset:1024\n"
      "ds_read_b64_tr_b16 v[122:123], v15 offset:1280\n"
      "s_waitcnt lgkmcnt(6)\n"
      "v_mfma_f32_32x32x16_bf16 v[16:31], v[104:107], v[188:191], v[16:31]\n"
      "ds_read_b64_tr_b16 v[64:65], v7 offset:4096\n"
      "ds_read_b64_tr_b16 v[66:67], v7 offset:4608\n"
      "v_mfma_f32_32x32x16_bf16 v[32:47], v[108:111], v[188:191], v[32:47]\n"
      "ds_read_b64_tr_b16 v[68:69], v8 offset:4096\n"
      "ds_read_b64_tr_b16 v[70:71], v8 offset:4608\n"
      "v_mfma_f32_32x32x16_bf16 v[48:63], v[112:115], v[188:191], v[48:63]\n"
      "ds_read_b64_tr_b16 v[72:73], v15 offset:2048\n"
      "ds_read_b64_tr_b16 v[74:75], v15 offset:2304\n"
      "s_waitcnt lgkmcnt(6)\n"
      "v_mfma_f32_32x32x16_bf16 v[16:31], v[76:79], v[192:195], v[16:31]\n"
      "ds_read_b128 v[88:91], v100\n"
      "ds_read_b64_tr_b16 v[104:105], v7 offset:6144\n"
      "ds_read_b64_tr_b16 v[106:107], v7 offset:6656\n"
      "v_mfma_f32_32x32x16_bf16 v[32:47], v[116:119], v[192:195], v[32:47]\n"
      "ds_read_b64_tr_b16 v[108:109], v8 offset:6144\n"
      "ds_read_b64_tr_b16 v[110:111], v8 offset:6656\n"
      "v_mfma_f32_32x32x16_bf16 v[48:63], v[120:123], v[192:195], v[48:63]\n"
      "ds_read_b64_tr_b16 v[112:113], v15 offset:3072\n"
      "ds_read_b64_tr_b16 v[114:115], v15 offset:3328\n"
      "s_waitcnt lgkmcnt(6)\n"
      "v_mfma_f32_32x32x16_bf16 v[16:31], v[64:67], v[196:199], v[16:31]\n"
      "v_min3_u32 v88, v88, v89, v90\n"
      "v_min_u32 v88, v88, v91\n"
      "v_cmp_le_u32 vcc, 2, v88\n"
      "s_cbranch_vccz .Llz_28_%=\n"
      ".Llzr_29_%=:\n"
      "ds_read_b64_tr_b16 v[76:77], v7 offset:12288\n"
      "ds_read_b64_tr_b16 v[78:79], v7 offset:12800\n"
      "v_mfma_f32_32x32x16_bf16 v[32:47], v[68:71], v[196:199], v[32:47]\n"
      "ds_read_b64_tr_b16 v[116:117], v8 offset:12288\n"
      "ds_read_b64_tr_b16 v[118:119], v8 offset:12800\n"
      "s_add_u32 m0, s96, 0\n"
      "s_nop 0\n"
      "buffer_load_dwordx4 v4, s[8:11], s31 offen offset:0 lds\n"
      "v_mfma_f32_32x32x16_bf16 v[48:63], v[72:75], v[196:199], v[48:63]\n"
      "ds_read_b64_tr_b16 v[120:121], v15 offset:12288\n"
      "ds_read_b64_tr_b16 v[122:123], v15 offset:12544\n"
      "s_waitcnt lgkmcnt(6)\n"
      "v_mfma_f32_32x32x16_bf16 v[16:31], v[104:107], v[200:203], v[16:31]\n"
      "ds_read_b64_tr_b16 v[64:65], v7 offset:14336\n"
      "ds_read_b64_tr_b16 v[66:67], v7 offset:14848\n"
      "v_mfma_f32_32x32x16_bf16 v[32:47], v[108:111], v[200:203], v[32:47]\n"
      "ds_read_b64_tr_b16 v[68:69], v8 offset:14336\n"
      "ds_read_b64_tr_b16 v[70:71], v8 offset:14848\n"
      "buffer_load_dwordx4 v96, s[8:11], s31 offen offset:1024 lds\n"
      "v_mfma_f32_32x32x16_bf16 v[48:63], v[112:115], v[200:203], v[48:63]\n"
      "ds_read_b64_tr_b16 v[72:73], v15 offset:13312\n"
      "ds_read_b64_tr_b16 v[74:75], v15 offset:13568\n"
      "buffer_load_dwordx4 v97, s[8:11], s31 offen offset:2048 lds\n"
      "s_waitcnt lgkmcnt(6)\n"
      "v_mfma_f32_32x32x16_bf16 v[16:31], v[76:79], v[204:207], v[16:31]\n"
      "ds_read_b64_tr_b16 v[104:105], v7 offset:16384\n"
      "ds_read_b64_tr_b16 v[106:107], v7 offset:16896\n"
      "v_mfma_f32_32x32x16_bf16 v[32:47], v[116:119], v[204:207], v[32:47]\n"
      "ds_read_b64_tr_b16 v[108:109], v8 offset:16384\n"
      "ds_read_b64_tr_b16 v[110:111], v8 offset:16896\n"
      "buffer_load_dwordx4 v98, s[8:11], s31 offen offset:3072 lds\n"
      "v_mfma_f32_32x32x16_bf16 v[48:63], v[120:123], v[204:207], v[48:63]\n"
      "ds_read_b64_tr_b16 v[112:113], v15 offset:14336\n"
      "ds_read_b64_tr_b16 v[114:115], v15 offset:14592\n"
      "v_mov_b32 v12, s58\n"
      "s_waitcnt lgkmcnt(6)\n"
      "v_mfma_f32_32x32x16_bf16 v[16:31], v[64:67], v[208:211], v[16:31]\n"
      "ds_read_b64_tr_b16 v[76:77], v7 offset:18432\n"
      "ds_read_b64_tr_b16 v[78:79], v7 offset:18944\n"
      "v_mfma_f32_32x32x16_bf16 v[32:47], v[68:71], v[208:211], v[32:47]\n"
      "ds_read_b64_tr_b16 v[116:117], v8 offset:18432\n"
      "ds_read_b64_tr_b16 v[118:119], v8 offset:18944\n"
      "s_add_u32 m0, s96, 4096\n"
      "s_nop 0\n"
      "buffer_load_dwordx4 v4, s[8:11], s0 offen offset:0 lds\n"
      "v_mfma_f32_32x32x16_bf16 v[48:63], v[72:75], v[208:211], v[48:63]\n"
      "ds_read_b64_tr_b16 v[120:121], v15 offset:15360\n"
      "ds_read_b64_tr_b16 v[122:123], v15 offset:15616\n"
      "buffer_load_dwordx4 v96, s[8:11], s0 offen offset:1024 lds\n"
      "s_waitcnt lgkmcnt(6)\n"
      "v_mfma_f32_32x32x16_bf16 v[16:31], v[104:107], v[212:215], v[16:31]\n"
      "ds_read_b64_tr_b16 v[64:65], v7 offset:24576\n"
      "ds_read_b64_tr_b16 v[66:67], v7 offset:25088\n"
      "v_mfma_f32_32x32x16_bf16 v[32:47], v[108:111], v[212:215], v[32:47]\n"
      "ds_read_b64_tr_b16 v[68:69], v8 offset:24576\n"
      "ds_read_b64_tr_b16 v[70:71], v8 offset:25088\n"
      "buffer_load_dwordx4 v97, s[8:11], s0 offen offset:2048 lds\n"
      "v_mfma_f32_32x32x16_bf16 v[48:63], v[112:115], v[212:215], v[48:63]\n"
      "ds_read_b64_tr_b16 v[72:73], v15 offset:24576\n"
      "ds_read_b64_tr_b16 v[74:75], v15 offset:24832\n"
      "v_mov_b32 v13, s59\n"
      "s_waitcnt lgkmcnt(6)\n"
      "v_mfma_f32_32x32x16_bf16 v[16:31], v[76:79], v[216:219], v[16:31]\n"
      "ds_read_b64_tr_b16 v[104:105], v7 offset:26624\n"
      "ds_read_b64_tr_b16 v[106:107], v7 offset:27136\n"
      "v_mfma_f32_32x32x16_bf16 v[32:47], v[116:119], v[216:219], v[32:47]\n"
      "ds_read_b64_tr_b16 v[108:109], v8 offset:26624\n"
      "ds_read_b64_tr_b16 v[110:111], v8 offset:27136\n"
      "buffer_load_dwordx4 v98, s[8:11], s0 offen offset:3072 lds\n"
      "v_mfma_f32_32x32x16_bf16 v[48:63], v[120:123], v[216:219], v[48:63]\n"
      "ds_read_b64_tr_b16 v[112:113], v15 offset:25600\n"
      "ds_read_b64_tr_b16 v[114:115], v15 offset:25856\n"
      "s_add_u32 m0, s96, 8192\n"
      "s_nop 0\n"
      "buffer_load_dwordx4 v4, s[8:11], s1 offen offset:0 lds\n"
      "s_waitcnt lgkmcnt(6)\n"
      "v_mfma_f32_32x32x16_bf16 v[16:31], v[64:67], v[220:223], v[16:31]\n"
      "ds_read_b64_tr_b16 v[76:77], v7 offset:28672\n"
      "ds_read_b64_tr_b16 v[78:79], v7 offset:29184\n"
      "v_mfma_f32_32x32x16_bf16 v[32:47], v[68:71], v[220:223], v[32:47]\n"
      "ds_read_b64_tr_b16 v[116:117], v8 offset:28672\n"
      "ds_read_b64_tr_b16 v[118:119], v8 offset:29184\n"
      "buffer_load_dwordx4 v96, s[8:11], s1 offen offset:1024 lds\n"
      "v_mfma_f32_32x32x16_bf16 v[48:63], v[72:75], v[220:223], v[48:63]\n"
      "ds_read_b64_tr_b16 v[120:121], v15 offset:26624\n"
      "ds_read_b64_tr_b16 v[122:123], v15 offset:26880\n"
      "s_and_b32 s69, s99, 2\n"
      "s_waitcnt lgkmcnt(6)\n"
      "v_mfma_f32_32x32x16_bf16 v[16:31], v[104:107], v[224:227], v[16:31]\n"
      "s_waitcnt vmcnt(6)\n"
      "ds_write_b32 v99, v101 offset:48\n"
      "ds_read_b64_tr_b16 v[64:65], v7 offset:30720\n"
      "ds_read_b64_tr_b16 v[66:67], v7 offset:31232\n"
      "v_mfma_f32_32x32x16_bf16 v[32:47], v[108:111], v[224:227], v[32:47]\n"
      "ds_read_b64_tr_b16 v[68:69], v8 offset:30720\n"
      "ds_read_b64_tr_b16 v[70:71], v8 offset:31232\n"
      "buffer_load_dwordx4 v97, s[8:11], s1 offen offset:2048 lds\n"
      "v_mfma_f32_32x32x16_bf16 v[48:63], v[112:115], v[224:227], v[48:63]\n"
      "ds_read_b64_tr_b16 v[72:73], v15 offset:27648\n"
      "ds_read_b64_tr_b16 v[74:75], v15 offset:27904\n"
      "buffer_load_dwordx4 v98, s[8:11], s1 offen offset:3072 lds\n"
      "s_waitcnt lgkmcnt(6)\n"
      "v_mfma_f32_32x32x16_bf16 v[16:31], v[76:79], v[228:231], v[16:31]\n"
      "ds_read_b64_tr_b16 v[104:105], v7 offset:36864\n"
      "ds_read_b64_tr_b16 v[106:107], v7 offset:37376\n"
      "v_mfma_f32_32x32x16_bf16 v[32:47], v[116:119], v[228:231], v[32:47]\n"
      "ds_read_b64_tr_b16 v[108:109], v8 offset:36864\n"
      "ds_read_b64_tr_b16 v[110:111], v8 offset:37376\n"
      "v_mfma_f32_32x32x16_bf16 v[48:63], v[120:123], v[228:231], v[48:63]\n"
      "ds_read_b64_tr_b16 v[112:113], v15 offset:36864\n"
      "ds_read_b64_tr_b16 v[114:115], v15 offset:37120\n"
      "s_mul_i32 s69, s69, 12288\n"
      "s_waitcnt lgkmcnt(6)\n"
      "v_mfma_f32_32x32x16_bf16 v[16:31], v[64:67], v[232:235], v[16:31]\n"
      "ds_read_b64_tr_b16 v[76:77], v7 offset:38912\n"
      "ds_read_b64_tr_b16 v[78:79], v7 offset:39424\n"
      "v_mfma_f32_32x32x16_bf16 v[32:47], v[68:71], v[232:235], v[32:47]\n"
      "ds_read_b64_tr_b16 v[116:117], v8 offset:38912\n"
      "ds_read_b64_tr_b16 v[118:119], v8 offset:39424\n"
      "v_mfma_f32_32x32x16_bf16 v[48:63], v[72:75], v[232:235], v[48:63]\n"
      "ds_read_b64_tr_b16 v[120:121], v15 offset:37888\n"
      "ds_read_b64_tr_b16 v[122:123], v15 offset:38144\n"
      "v_lshlrev_b32 v1, 4, v0\n"
      "s_waitcnt lgkmcnt(6)\n"
      "v_mfma_f32_32x32x16_bf16 v[16:31], v[104:107], v[236:239], v[16:31]\n"
      "ds_read_b64_tr_b16 v[64:65], v7 offset:40960\n"
      "ds_read_b64_tr_b16 v[66:67], v7 offset:41472\n"
      "v_mfma_f32_32x32x16_bf16 v[32:47], v[108:111], v[236:239], v[32:47]\n"
      "ds_read_b64_tr_b16 v[68:69], v8 offset:40960\n"
      "ds_read_b64_tr_b16 v[70:71], v8 offset:41472\n"
      "v_mfma_f32_32x32x16_bf16 v[48:63], v[112:115], v[236:239], v[48:63]\n"
      "ds_read_b64_tr_b16 v[72:73], v15 offset:38912\n"
      "ds_read_b64_tr_b16 v[74:75], v15 offset:39168\n"
      "v_add_u32 v2, s69, v253\n"
      "s_waitcnt lgkmcnt(6)\n"
      "v_mfma_f32_32x32x16_bf16 v[16:31], v[76:79], v[240:243], v[16:31]\n"
      "ds_read_b64_tr_b16 v[104:105], v7 offset:43008\n"
      "ds_read_b64_tr_b16 v[106:107], v7 offset:43520\n"
      "v_mfma_f32_32x32x16_bf16 v[32:47], v[116:119], v[240:243], v[32:47]\n"
      "ds_read_b64_tr_b16 v[108:109], v8 offset:43008\n"
      "ds_read_b64_tr_b16 v[110:111], v8 offset:43520\n"
      "v_mfma_f32_32x32x16_bf16 v[48:63], v[120:123], v[240:243], v[48:63]\n"
      "ds_read_b64_tr_b16 v[112:113], v15 offset:39936\n"
      "ds_read_b64_tr_b16 v[114:115], v15 offset:40192\n"
      "v_xor_b32 v3, 64, v2\n"
      "s_waitcnt lgkmcnt(6)\n"
      "v_mfma_f32_32x32x16_bf16 v[16:31], v[64:67], v[244:247], v[16:31]\n"
      "v_mfma_f32_32x32x16_bf16 v[32:47], v[68:71], v[244:247], v[32:47]\n"
      "v_mfma_f32_32x32x16_bf16 v[48:63], v[72:75], v[244:247], v[48:63]\n"
      "v_add_u32 v5, 49152, v2\n"
      "s_waitcnt lgkmcnt(0)\n"
      "v_mfma_f32_32x32x16_bf16 v[16:31], v[104:107], v[248:251], v[16:31]\n"
      "ds_write_b32 v99, v103\n"
      "v_mfma_f32_32x32x16_bf16 v[32:47], v[108:111], v[248:251], v[32:47]\n"
      "v_mfma_f32_32x32x16_bf16 v[48:63], v[112:115], v[248:251], v[48:63]\n"
      "v_add_u32 v6, 49152, v3\n"
      "s_mov_b32 s68, 0x358637bd\n"
      "s_mov_b32 s69, 0x358637bd\n"
      "v_lshlrev_b32 v7, 10, v0\n"
      "s_setprio 2\n"
      "s_nop 3\n"
      "v_pk_mul_f32 v[152:153], v[16:17], s[60:61]\n"
      "v_pk_mul_f32 v[154:155], v[18:19], s[60:61]\n"
      "v_pk_mul_f32 v[156:157], v[20:21], s[60:61]\n"
      "v_pk_mul_f32 v[158:159], v[22:23], s[60:61]\n"
      "v_pk_mul_f32 v[160:161], v[24:25], s[60:61]\n"
      "v_pk_mul_f32 v[162:163], v[26:27], s[60:61]\n"
      "v_pk_mul_f32 v[164:165], v[28:29], s[60:61]\n"
      "v_pk_mul_f32 v[166:167], v[30:31], s[60:61]\n"
      "v_exp_f32 v152, v152\n"
      "v_exp_f32 v153, v153\n"
      "v_exp_f32 v154, v154\n"
      "v_exp_f32 v155, v155\n"
      "v_exp_f32 v156, v156\n"
      "v_exp_f32 v157, v157\n"
      "v_exp_f32 v158, v158\n"
      "v_exp_f32 v159, v159\n"
      "v_exp_f32 v160, v160\n"
      "v_exp_f32 v161, v161\n"
      "v_exp_f32 v162, v162\n"
      "v_exp_f32 v163, v163\n"
      "v_exp_f32 v164, v164\n"
      "v_exp_f32 v165, v165\n"
      "v_exp_f32 v166, v166\n"
      "v_exp_f32 v167, v167\n"
      "v_pk_fma_f32 v[168:169], v[152:153], 0.5, 1.0 op_sel_hi:[1,0,0]\n"
      "v_pk_fma_f32 v[170:171], v[154:155], 0.5, 1.0 op_sel_hi:[1,0,0]\n"
      "v_pk_fma_f32 v[172:173], v[156:157], 0.5, 1.0 op_sel_hi:[1,0,0]\n"
      "v_pk_fma_f32 v[174:175], v[158:159], 0.5, 1.0 op_sel_hi:[1,0,0]\n"
      "v_pk_fma_f32 v[176:177], v[160:161], 0.5, 1.0 op_sel_hi:[1,0,0]\n"
      "v_pk_fma_f32 v[178:179], v[162:163], 0.5, 1.0 op_sel_hi:[1,0,0]\n"
      "v_pk_fma_f32 v[180:181], v[164:165], 0.5, 1.0 op_sel_hi:[1,0,0]\n"
      "v_pk_fma_f32 v[182:183], v[166:167], 0.5, 1.0 op_sel_hi:[1,0,0]\n"
      "v_pk_fma_f32 v[152:153], v[152:153], v[168:169], 1.0 op_sel_hi:[1,1,0]\n"
      "v_pk_fma_f32 v[154:155], v[154:155], v[170:171], 1.0 op_sel_hi:[1,1,0]\n"
      "v_pk_fma_f32 v[156:157], v[156:157], v[172:173], 1.0 op_sel_hi:[1,1,0]\n"
      "v_pk_fma_f32 v[158:159], v[158:159], v[174:175], 1.0 op_sel_hi:[1,1,0]\n"
      "v_pk_fma_f32 v[160:161], v[160:161], v[176:177], 1.0 op_sel_hi:[1,1,0]\n"
      "v_pk_fma_f32 v[162:163], v[162:163], v[178:179], 1.0 op_sel_hi:[1,1,0]\n"
      "v_pk_fma_f32 v[164:165], v[164:165], v[180:181], 1.0 op_sel_hi:[1,1,0]\n"
      "v_pk_fma_f32 v[166:167], v[166:167], v[182:183], 1.0 op_sel_hi:[1,1,0]\n"
      "v_rcp_f32 v152, v152\n"
      "v_rcp_f32 v153, v153\n"
      "v_rcp_f32 v154, v154\n"
      "v_rcp_f32 v155, v155\n"
      "v_rcp_f32 v156, v156\n"
      "v_rcp_f32 v157, v157\n"
      "v_rcp_f32 v158, v158\n"
      "v_rcp_f32 v159, v159\n"
      "v_rcp_f32 v160, v160\n"
      "v_rcp_f32 v161, v161\n"
      "v_rcp_f32 v162, v162\n"
      "v_rcp_f32 v163, v163\n"
      "v_rcp_f32 v164, v164\n"
      "v_rcp_f32 v165, v165\n"
      "v_rcp_f32 v166, v166\n"
      "v_rcp_f32 v167, v167\n"
      "v_pk_fma_f32 v[168:169], v[16:17], v[152:153], v[16:17] neg_lo:[1,0,0] neg_hi:[1,0,0]\n"
      "v_pk_fma_f32 v[170:171], v[18:19], v[154:155], v[18:19] neg_lo:[1,0,0] neg_hi:[1,0,0]\n"
      "v_pk_fma_f32 v[172:173], v[20:21], v[156:157], v[20:21] neg_lo:[1,0,0] neg_hi:[1,0,0]\n"
      "v_pk_fma_f32 v[174:175], v[22:23], v[158:159], v[22:23] neg_lo:[1,0,0] neg_hi:[1,0,0]\n"
      "v_pk_fma_f32 v[176:177], v[24:25], v[160:161], v[24:25] neg_lo:[1,0,0] neg_hi:[1,0,0]\n"
      "v_pk_fma_f32 v[178:179], v[26:27], v[162:163], v[26:27] neg_lo:[1,0,0] neg_hi:[1,0,0]\n"
      "v_pk_fma_f32 v[180:181], v[28:29], v[164:165], v[28:29] neg_lo:[1,0,0] neg_hi:[1,0,0]\n"
      "v_pk_fma_f32 v[182:183], v[30:31], v[166:167], v[30:31] neg_lo:[1,0,0] neg_hi:[1,0,0]\n"
      "v_cvt_pk_bf16_f32 v216, v168, v169\n"
      "v_cvt_pk_bf16_f32 v217, v170, v171\n"
      "v_cvt_pk_bf16_f32 v218, v172, v173\n"
      "v_cvt_pk_bf16_f32 v219, v174, v175\n"
      "v_cvt_pk_bf16_f32 v220, v176, v177\n"
      "v_cvt_pk_bf16_f32 v221, v178, v179\n"
      "v_cvt_pk_bf16_f32 v222, v180, v181\n"
      "v_cvt_pk_bf16_f32 v223, v182, v183\n"
      "s_setprio 3\n"
      "buffer_store_dwordx4 v[216:219], v1, s[16:19], s40 offen sc1\n"
      "buffer_store_dwordx4 v[220:223], v1, s[16:19], s40 offen offset:1024 sc1\n"
      "s_setprio 2\n"
      "v_pk_mul_f32 v[152:153], v[32:33], s[60:61]\n"
      "v_pk_mul_f32 v[154:155], v[34:35], s[60:61]\n"
      "v_pk_mul_f32 v[156:157], v[36:37], s[60:61]\n"
      "v_pk_mul_f32 v[158:159], v[38:39], s[60:61]\n"
      "v_pk_mul_f32 v[160:161], v[40:41], s[60:61]\n"
      "v_pk_mul_f32 v[162:163], v[42:43], s[60:61]\n"
      "v_pk_mul_f32 v[164:165], v[44:45], s[60:61]\n"
      "v_pk_mul_f32 v[166:167], v[46:47], s[60:61]\n"
      "v_exp_f32 v152, v152\n"
      "v_exp_f32 v153, v153\n"
      "v_exp_f32 v154, v154\n"
      "v_exp_f32 v155, v155\n"
      "v_exp_f32 v156, v156\n"
      "v_exp_f32 v157, v157\n"
      "v_exp_f32 v158, v158\n"
      "v_exp_f32 v159, v159\n"
      "v_exp_f32 v160, v160\n"
      "v_exp_f32 v161, v161\n"
      "v_exp_f32 v162, v162\n"
      "v_exp_f32 v163, v163\n"
      "v_exp_f32 v164, v164\n"
      "v_exp_f32 v165, v165\n"
      "v_exp_f32 v166, v166\n"
      "v_exp_f32 v167, v167\n"
      "v_pk_fma_f32 v[168:169], v[152:153], 0.5, 1.0 op_sel_hi:[1,0,0]\n"
      "v_pk_fma_f32 v[170:171], v[154:155], 0.5, 1.0 op_sel_hi:[1,0,0]\n"
      "v_pk_fma_f32 v[172:173], v[156:157], 0.5, 1.0 op_sel_hi:[1,0,0]\n"
      "v_pk_fma_f32 v[174:175], v[158:159], 0.5, 1.0 op_sel_hi:[1,0,0]\n"
      "v_pk_fma_f32 v[176:177], v[160:161], 0.5, 1.0 op_sel_hi:[1,0,0]\n"
      "v_pk_fma_f32 v[178:179], v[162:163], 0.5, 1.0 op_sel_hi:[1,0,0]\n"
      "v_pk_fma_f32 v[180:181], v[164:165], 0.5, 1.0 op_sel_hi:[1,0,0]\n"
      "v_pk_fma_f32 v[182:183], v[166:167], 0.5, 1.0 op_sel_hi:[1,0,0]\n"
      "v_pk_fma_f32 v[152:153], v[152:153], v[168:169], 1.0 op_sel_hi:[1,1,0]\n"
      "v_pk_fma_f32 v[154:155], v[154:155], v[170:171], 1.0 op_sel_hi:[1,1,0]\n"
      "v_pk_fma_f32 v[156:157], v[156:157], v[172:173], 1.0 op_sel_hi:[1,1,0]\n"
      "v_pk_fma_f32 v[158:159], v[158:159], v[174:175], 1.0 op_sel_hi:[1,1,0]\n"
      "v_pk_fma_f32 v[160:161], v[160:161], v[176:177], 1.0 op_sel_hi:[1,1,0]\n"
      "v_pk_fma_f32 v[162:163], v[162:163], v[178:179], 1.0 op_sel_hi:[1,1,0]\n"
      "v_pk_fma_f32 v[164:165], v[164:165], v[180:181], 1.0 op_sel_hi:[1,1,0]\n"
      "v_pk_fma_f32 v[166:167], v[166:167], v[182:183], 1.0 op_sel_hi:[1,1,0]\n"
      "v_rcp_f32 v152, v152\n"
      "v_rcp_f32 v153, v153\n"
      "v_rcp_f32 v154, v154\n"
      "v_rcp_f32 v155, v155\n"
      "v_rcp_f32 v156, v156\n"
      "v_rcp_f32 v157, v157\n"
      "v_rcp_f32 v158, v158\n"
      "v_rcp_f32 v159, v159\n"
      "v_rcp_f32 v160, v160\n"
      "v_rcp_f32 v161, v161\n"
      "v_rcp_f32 v162, v162\n"
      "v_rcp_f32 v163, v163\n"
      "v_rcp_f32 v164, v164\n"
      "v_rcp_f32 v165, v165\n"
      "v_rcp_f32 v166, v166\n"
      "v_rcp_f32 v167, v167\n"
      "v_pk_fma_f32 v[168:169], v[32:33], v[152:153], v[32:33] neg_lo:[1,0,0] neg_hi:[1,0,0]\n"
      "v_pk_fma_f32 v[170:171], v[34:35], v[154:155], v[34:35] neg_lo:[1,0,0] neg_hi:[1,0,0]\n"
      "v_pk_fma_f32 v[172:173], v[36:37], v[156:157], v[36:37] neg_lo:[1,0,0] neg_hi:[1,0,0]\n"
      "v_pk_fma_f32 v[174:175], v[38:39], v[158:159], v[38:39] neg_lo:[1,0,0] neg_hi:[1,0,0]\n"
      "v_pk_fma_f32 v[176:177], v[40:41], v[160:161], v[40:41] neg_lo:[1,0,0] neg_hi:[1,0,0]\n"
      "v_pk_fma_f32 v[178:179], v[42:43], v[162:163], v[42:43] neg_lo:[1,0,0] neg_hi:[1,0,0]\n"
      "v_pk_fma_f32 v[180:181], v[44:45], v[164:165], v[44:45] neg_lo:[1,0,0] neg_hi:[1,0,0]\n"
      "v_pk_fma_f32 v[182:183], v[46:47], v[166:167], v[46:47] neg_lo:[1,0,0] neg_hi:[1,0,0]\n"
      "v_cvt_pk_bf16_f32 v224, v168, v169\n"
      "v_cvt_pk_bf16_f32 v225, v170, v171\n"
      "v_cvt_pk_bf16_f32 v226, v172, v173\n"
      "v_cvt_pk_bf16_f32 v227, v174, v175\n"
      "v_cvt_pk_bf16_f32 v228, v176, v177\n"
      "v_cvt_pk_bf16_f32 v229, v178, v179\n"
      "v_cvt_pk_bf16_f32 v230, v180, v181\n"
      "v_cvt_pk_bf16_f32 v231, v182, v183\n"
      "s_setprio 3\n"
      "buffer_store_dwordx4 v[224:227], v1, s[16:19], s41 offen sc1\n"
      "buffer_store_dwordx4 v[228:231], v1, s[16:19], s41 offen offset:1024 sc1\n"
      "s_setprio 2\n"
      "v_pk_mul_f32 v[152:153], v[48:49], s[60:61]\n"
      "v_pk_mul_f32 v[154:155], v[50:51], s[60:61]\n"
      "v_pk_mul_f32 v[156:157], v[52:53], s[60:61]\n"
      "v_pk_mul_f32 v[158:159], v[54:55], s[60:61]\n"
      "v_pk_mul_f32 v[160:161], v[56:57], s[60:61]\n"
      "v_pk_mul_f32 v[162:163], v[58:59], s[60:61]\n"
      "v_pk_mul_f32 v[164:165], v[60:61], s[60:61]\n"
      "v_pk_mul_f32 v[166:167], v[62:63], s[60:61]\n"
      "v_exp_f32 v152, v152\n"
      "v_exp_f32 v153, v153\n"
      "v_exp_f32 v154, v154\n"
      "v_exp_f32 v155, v155\n"
      "v_exp_f32 v156, v156\n"
      "v_exp_f32 v157, v157\n"
      "v_exp_f32 v158, v158\n"
      "v_exp_f32 v159, v159\n"
      "v_exp_f32 v160, v160\n"
      "v_exp_f32 v161, v161\n"
      "v_exp_f32 v162, v162\n"
      "v_exp_f32 v163, v163\n"
      "v_exp_f32 v164, v164\n"
      "v_exp_f32 v165, v165\n"
      "v_exp_f32 v166, v166\n"
      "v_exp_f32 v167, v167\n"
      "v_pk_fma_f32 v[168:169], v[152:153], 0.5, 1.0 op_sel_hi:[1,0,0]\n"
      "v_pk_fma_f32 v[170:171], v[154:155], 0.5, 1.0 op_sel_hi:[1,0,0]\n"
      "v_pk_fma_f32 v[172:173], v[156:157], 0.5, 1.0 op_sel_hi:[1,0,0]\n"
      "v_pk_fma_f32 v[174:175], v[158:159], 0.5, 1.0 op_sel_hi:[1,0,0]\n"
      "v_pk_fma_f32 v[176:177], v[160:161], 0.5, 1.0 op_sel_hi:[1,0,0]\n"
      "v_pk_fma_f32 v[178:179], v[162:163], 0.5, 1.0 op_sel_hi:[1,0,0]\n"
      "v_pk_fma_f32 v[180:181], v[164:165], 0.5, 1.0 op_sel_hi:[1,0,0]\n"
      "v_pk_fma_f32 v[182:183], v[166:167], 0.5, 1.0 op_sel_hi:[1,0,0]\n"
      "v_pk_fma_f32 v[152:153], v[152:153], v[168:169], 1.0 op_sel_hi:[1,1,0]\n"
      "v_pk_fma_f32 v[154:155], v[154:155], v[170:171], 1.0 op_sel_hi:[1,1,0]\n"
      "v_pk_fma_f32 v[156:157], v[156:157], v[172:173], 1.0 op_sel_hi:[1,1,0]\n"
      "v_pk_fma_f32 v[158:159], v[158:159], v[174:175], 1.0 op_sel_hi:[1,1,0]\n"
      "v_pk_fma_f32 v[160:161], v[160:161], v[176:177], 1.0 op_sel_hi:[1,1,0]\n"
      "v_pk_fma_f32 v[162:163], v[162:163], v[178:179], 1.0 op_sel_hi:[1,1,0]\n"
      "v_pk_fma_f32 v[164:165], v[164:165], v[180:181], 1.0 op_sel_hi:[1,1,0]\n"
      "v_pk_fma_f32 v[166:167], v[166:167], v[182:183], 1.0 op_sel_hi:[1,1,0]\n"
      "v_rcp_f32 v152, v152\n"
      "v_rcp_f32 v153, v153\n"
      "v_rcp_f32 v154, v154\n"
      "v_rcp_f32 v155, v155\n"
      "v_rcp_f32 v156, v156\n"
      "v_rcp_f32 v157, v157\n"
      "v_rcp_f32 v158, v158\n"
      "v_rcp_f32 v159, v159\n"
      "v_rcp_f32 v160, v160\n"
      "v_rcp_f32 v161, v161\n"
      "v_rcp_f32 v162, v162\n"
      "v_rcp_f32 v163, v163\n"
      "v_rcp_f32 v164, v164\n"
      "v_rcp_f32 v165, v165\n"
      "v_rcp_f32 v166, v166\n"
      "v_rcp_f32 v167, v167\n"
      "v_pk_fma_f32 v[168:169], v[48:49], v[152:153], v[48:49] neg_lo:[1,0,0] neg_hi:[1,0,0]\n"
      "v_pk_fma_f32 v[170:171], v[50:51], v[154:155], v[50:51] neg_lo:[1,0,0] neg_hi:[1,0,0]\n"
      "v_pk_fma_f32 v[172:173], v[52:53], v[156:157], v[52:53] neg_lo:[1,0,0] neg_hi:[1,0,0]\n"
      "v_pk_fma_f32 v[174:175], v[54:55], v[158:159], v[54:55] neg_lo:[1,0,0] neg_hi:[1,0,0]\n"
      "v_pk_fma_f32 v[176:177], v[56:57], v[160:161], v[56:57] neg_lo:[1,0,0] neg_hi:[1,0,0]\n"
      "v_pk_fma_f32 v[178:179], v[58:59], v[162:163], v[58:59] neg_lo:[1,0,0] neg_hi:[1,0,0]\n"
      "v_pk_fma_f32 v[180:181], v[60:61], v[164:165], v[60:61] neg_lo:[1,0,0] neg_hi:[1,0,0]\n"
      "v_pk_fma_f32 v[182:183], v[62:63], v[166:167], v[62:63] neg_lo:[1,0,0] neg_hi:[1,0,0]\n"
      "v_cvt_pk_bf16_f32 v216, v168, v169\n"
      "v_cvt_pk_bf16_f32 v217, v170, v171\n"
      "v_cvt_pk_bf16_f32 v218, v172, v173\n"
      "v_cvt_pk_bf16_f32 v219, v174, v175\n"
      "v_cvt_pk_bf16_f32 v220, v176, v177\n"
      "v_cvt_pk_bf16_f32 v221, v178, v179\n"
      "v_cvt_pk_bf16_f32 v222, v180, v181\n"
      "v_cvt_pk_bf16_f32 v223, v182, v183\n"
      "s_setprio 3\n"
      "buffer_store_dwordx4 v[216:219], v1, s[16:19], s42 offen sc1\n"
      "buffer_store_dwordx4 v[220:223], v1, s[16:19], s42 offen offset:1024 sc1\n"
      "s_branch .LPB_4_%=\n"
      ".Llz_24_%=:\n"
      "v_mov_b32 v90, 160784\n"
      "s_mov_b32 s76, 0x400000\n"
      ".Llw_26_%=:\n"
      "ds_read_b128 v[92:95], v90\n"
      "s_waitcnt lgkmcnt(0)\n"
      "v_min3_u32 v92, v92, v93, v94\n"
      "v_min_u32 v92, v92, v95\n"
      "v_cmp_le_u32 vcc, 1, v92\n"
      "s_cbranch_vccnz .Llwx_27_%=\n"
      "s_sub_u32 s76, s76, 1\n"
      "s_cbranch_scc0 .Llw_26_%=\n"
      ".Llwx_27_%=:\n"
      "s_branch .Llzr_25_%=\n"
      ".Llz_28_%=:\n"
      "v_mov_b32 v90, 160784\n"
      "s_mov_b32 s76, 0x400000\n"
      ".Llw_30_%=:\n"
      "ds_read_b128 v[92:95], v90\n"
      "s_waitcnt lgkmcnt(0)\n"
      "v_min3_u32 v92, v92, v93, v94\n"
      "v_min_u32 v92, v92, v95\n"
      "v_cmp_le_u32 vcc, 2, v92\n"
      "s_cbranch_vccnz .Llwx_31_%=\n"
      "s_sub_u32 s76, s76, 1\n"
      "s_cbranch_scc0 .Llw_30_%=\n"
      ".Llwx_31_%=:\n"
      "s_branch .Llzr_29_%=\n"
      ".LA0_2_%=:\n"
      "s_and_b32 s64, s32, 1\n"
      "s_lshl_b32 s64, s64, 6\n"
      "s_add_u32 s72, s72, s64\n"
      "v_mov_b32 v121, 0\n"
      "v_lshrrev_b32 v9, 5, v0\n"
      "v_and_b32 v10, 31, v0\n"
      "v_bfe_u32 v11, v0, 2, 2\n"
      "v_bfe_u32 v12, v0, 1, 1\n"
      "v_bfe_u32 v13, v0, 4, 1\n"
      "v_lshl_or_b32 v12, v13, 1, v12\n"
      "v_and_b32 v14, 2, v11\n"
      "v_lshl_or_b32 v12, v14, 1, v12\n"
      "v_and_b32 v14, 1, v0\n"
      "v_lshlrev_b32 v14, 3, v14\n"
      "v_lshl_or_b32 v253, v12, 4, v14\n"
      "v_bfe_u32 v14, v0, 1, 3\n"
      "v_xor_b32 v14, v14, v9\n"
      "v_lshlrev_b32 v14, 4, v14\n"
      "v_lshl_or_b32 v5, v10, 7, v14\n"
      "v_lshl_or_b32 v14, v9, 3, v11\n"
      "v_lshl_or_b32 v6, v14, 7, v253\n"
      "v_lshl_or_b32 v12, v9, 2, v11\n"
      "v_lshl_or_b32 v253, v12, 7, v253\n"
      "s_mov_b32 s60, 0x3fb8aa3b\n"
      "s_mov_b32 s61, 0x3fb8aa3b\n"
      "s_bfe_u32 s63, s2, 0x20003\n"
      "s_bfe_u32 s64, s2, 0x20001\n"
      "s_lshl2_add_u32 s63, s64, s63\n"
      "s_and_b32 s64, s2, 1\n"
      "s_lshl1_add_u32 s36, s63, s64\n"
      "s_lshl2_add_u32 s98, s64, s99\n"
      "s_lshr_b32 s37, s2, 5\n"
      "s_xor_b32 s37, s37, 2\n"
      "s_mul_i32 s96, s99, 12288\n"
      "s_and_b32 s98, s98, 6\n"
      "s_lshl_b32 s64, s37, 7\n"
      "s_mul_i32 s39, s98, 229376\n"
      "s_add_u32 s39, s39, s64\n"
      "s_lshl_b32 s3, s98, 16\n"
      "s_add_u32 s3, s3, s64\n"
      "s_mov_b32 s50, 0\n"
      "s_mov_b32 s51, 0\n"
      "s_lshl3_add_u32 s54, s36, s37\n"
      "s_lshl3_add_u32 s54, s54, s32\n"
      "s_lshl_b32 s54, s54, 7\n"
      "s_add_u32 s54, s54, 15728640\n"
      "v_lshlrev_b32 v252, 3, v0\n"
      "v_cmp_gt_u32 vcc, 32, v0\n"
      "s_waitcnt lgkmcnt(0)\n"
      "s_cmp_lg_u32 s33, 0\n"
      "s_cbranch_scc1 .Lnb_32_%=\n"
      "v_lshlrev_b32 v9, 1, v0\n"
      "s_lshl_b32 s64, s72, 1\n"
      "buffer_load_ushort v121, v9, s[28:31], s64 offen\n"
      ".Lnb_32_%=:\n"
      "buffer_load_dword v120, off, s[16:19], 0 sc1\n"
      "s_cmp_lg_u32 s32, 0\n"
      "s_cbranch_scc1 .Lng_33_%=\n"
      "v_lshlrev_b32 v9, 4, v0\n"
      "s_mov_b32 m0, 147456\n"
      "s_nop 0\n"
      "buffer_load_dwordx4 v9, s[28:31], 0 offen lds\n"
      ".Lng_33_%=:\n"
      "s_cmp_lg_u32 s32, 1\n"
      "s_cbranch_scc1 .Lncp_34_%=\n"
      "s_getpc_b64 s[70:71]\n"
      ".Lpc_35_%=:\n"
      "s_sub_u32 s70, s70, .Lpc_35_%=-.Lstart_%=\n"
      "s_subb_u32 s71, s71, 0\n"
      "s_lshr_b32 s64, s2, 3\n"
      "s_mul_i32 s64, s64, 8\n"
      "v_add_u32 v9, s64, v0\n"
      "v_lshlrev_b32 v9, 7, v9\n"
      "v_min_u32 v9, .Lcodeend_1_%=-.Lstart_%=, v9\n"
      "s_mov_b64 exec, 0xff\n"
      "global_load_dword v125, v9, s[70:71]\n"
      "s_mov_b64 exec, -1\n"
      ".Lncp_34_%=:\n"
      "s_lshr_b32 s64, s32, 1\n"
      "s_lshl_b32 s64, s64, 12\n"
      "v_add_u32 v5, s64, v5\n"
      "s_and_b32 s64, s32, 1\n"
      "s_lshl_b32 s65, s64, 13\n"
      "s_lshl_b32 s64, s64, 6\n"
      "s_cmp_eq_u32 s33, 0\n"
      "s_cselect_b32 s65, s65, 0\n"
      "s_cselect_b32 s64, 0, s64\n"
      "s_cselect_b32 s68, 64, 0x2000\n"
      "s_add_u32 s65, s65, 16384\n"
      "v_xor_b32 v6, s64, v6\n"
      "v_add_u32 v6, s65, v6\n"
      "v_xor_b32 v7, s68, v6\n"
      "v_mov_b64 v[100:101], 0\n"
      "v_mov_b64 v[102:103], 0\n"
      "v_mov_b64 v[104:105], 0\n"
      "v_mov_b64 v[106:107], 0\n"
      "v_mov_b64 v[108:109], 0\n"
      "v_mov_b64 v[110:111], 0\n"
      "v_mov_b64 v[112:113], 0\n"
      "v_mov_b64 v[114:115], 0\n"
      "s_mov_b32 s52, 0\n"
      "s_mov_b32 s56, 0\n"
      "v_mov_b32 v9, v5\n"
      "v_xor_b32 v10, 32, v5\n"
      "v_mov_b32 v13, v6\n"
      "v_mov_b32 v14, v7\n"
      "s_and_b32 s69, s99, 2\n"
      "s_mul_i32 s69, s69, 12288\n"
      "v_lshlrev_b32 v1, 4, v0\n"
      "v_add_u32 v2, s69, v253\n"
      "v_xor_b32 v3, 64, v2\n"
      "s_and_b32 s65, s99, 1\n"
      "s_xor_b32 s62, s36, 1\n"
      "s_lshl1_add_u32 s62, s62, s65\n"
      "s_mul_i32 s62, s62, 147456\n"
      "s_mul_i32 s63, s98, 14336\n"
      "s_add_u32 s63, s63, s62\n"
      "s_add_u32 s46, s63, 1048576\n"
      "s_xor_b32 s62, s36, 0\n"
      "s_lshl1_add_u32 s62, s62, s65\n"
      "s_mul_i32 s62, s62, 147456\n"
      "s_mul_i32 s63, s98, 14336\n"
      "s_add_u32 s63, s63, s62\n"
      "s_add_u32 s48, s63, 1048576\n"
      "s_and_b32 s71, s99, 1\n"
      "s_lshl_b32 s28, s71, 15\n"
      "s_add_u32 s28, s28, s39\n"
      "s_add_u32 s28, s28, 196608\n"
      "s_lshl_b32 s29, s71, 16\n"
      "s_add_u32 s29, s29, s3\n"
      "s_add_u32 s29, s29, 0\n"
      "s_lshl_b32 s30, s71, 16\n"
      "s_add_u32 s30, s30, s3\n"
      "s_add_u32 s30, s30, 32768\n"
      "s_xor_b32 s31, s29, 0x40000\n"
      "s_and_b32 s62, s99, 2\n"
      "s_mul_i32 s62, s62, 12288\n"
      "s_bitcmp1_b32 s99, 0\n"
      "s_cselect_b32 s0, 8192, 0\n"
      "s_cselect_b32 s1, 0, 102400\n"
      "s_add_u32 s0, s0, s62\n"
      "s_add_u32 s0, s0, 98304\n"
      "s_add_u32 s1, s1, s62\n"
      "s_waitcnt vmcnt(0)\n"
      "v_readfirstlane_b32 s59, v120\n"
      "s_nop 0\n"
      "s_add_u32 s59, s59, 1\n"
      "s_not_b32 s58, s59\n"
      "v_mov_b32 v100, 0x3f80\n"
      "v_cndmask_b32 v108, 0, v121, vcc\n"
      "v_sub_u32 v112, v121, v108\n"
      "s_cmp_lg_u32 s32, 0\n"
      "s_cbranch_scc1 .Lnw0_36_%=\n"
      "v_mov_b32 v122, 160768\n"
      "v_mov_b64 v[128:129], 0\n"
      "v_mov_b64 v[130:131], 0\n"
      "ds_write_b128 v122, v[128:131]\n"
      "ds_write_b128 v122, v[128:131] offset:16\n"
      "ds_write_b128 v122, v[128:131] offset:48\n"
      "ds_write_b128 v122, v[128:131] offset:64\n"
      "ds_write_b128 v122, v[128:131] offset:80\n"
      "ds_write_b32 v122, v120 offset:32\n"
      ".Lnw0_36_%=:\n"
      "v_mov_b32 v126, 0\n"
      "v_lshrrev_b32 v127, 1, v0\n"
      "v_and_b32 v124, 1, v0\n"
      "v_lshlrev_b32 v123, 9, v124\n"
      "v_lshl_or_b32 v127, v127, 18, v123\n"
      "s_mov_b64 exec, 31\n"
      "buffer_atomic_or v126, v127, s[16:19], s54 offen offset:0 sc1\n"
      "s_cmp_eq_u32 s33, 0\n"
      "s_cbranch_scc1 .Lnwm_37_%=\n"
      "s_lshl2_add_u32 s64, s35, s99\n"
      "s_lshl4_add_u32 s64, s64, s34\n"
      "s_lshl_b32 s64, s64, 7\n"
      "s_add_u32 s64, s64, 4096\n"
      "v_lshlrev_b32 v123, 6, v124\n"
      "s_mov_b64 exec, 3\n"
      "buffer_atomic_or v126, v123, s[16:19], s64 offen sc1\n"
      ".Lnwm_37_%=:\n"
      "s_mov_b64 exec, -1\n"
      "v_mfma_f32_32x32x16_bf16 v[16:31], v[108:111], v[100:103], 0\n"
      "v_mfma_f32_32x32x16_bf16 v[48:63], v[108:111], v[100:103], 0\n"
      "v_mfma_f32_32x32x16_bf16 v[32:47], v[112:115], v[100:103], 0\n"
      "v_mfma_f32_32x32x16_bf16 v[64:79], v[112:115], v[100:103], 0\n"
      "s_cmp_lg_u32 s33, 0\n"
      "s_cbranch_scc1 .LQA_6_%=\n"
      "s_lshr_b32 s64, s32, 1\n"
      "s_xor_b32 s63, s64, s45\n"
      "s_lshl2_add_u32 s63, s34, s63\n"
      "s_mul_i32 s63, s63, 147456\n"
      "s_add_u32 s63, s63, 1048576\n"
      "s_lshl_b32 s62, s72, 6\n"
      "s_add_u32 s40, s63, s62\n"
      "s_add_u32 s41, s40, 2048\n"
      "s_xor_b32 s64, s64, 2\n"
      "s_xor_b32 s63, s64, s45\n"
      "s_lshl2_add_u32 s63, s34, s63\n"
      "s_mul_i32 s63, s63, 147456\n"
      "s_add_u32 s63, s63, 1048576\n"
      "s_add_u32 s42, s63, s62\n"
      "s_add_u32 s43, s42, 2048\n"
      "s_lshr_b32 s62, s35, 2\n"
      "s_lshl1_add_u32 s62, s34, s62\n"
      "s_lshl_b32 s45, s62, 13\n"
      "s_and_b32 s62, s35, 2\n"
      "s_lshl_b32 s62, s62, 7\n"
      "s_add_u32 s45, s45, s62\n"
      "s_and_b32 s62, s35, 1\n"
      "s_lshl4_add_u32 s45, s62, s45\n"
      "s_and_b32 s62, s99, 1\n"
      "s_lshl_b32 s62, s62, 3\n"
      "s_add_u32 s45, s45, s62\n"
      "s_lshr_b32 s62, s99, 1\n"
      "s_lshl_b32 s62, s62, 7\n"
      "s_add_u32 s45, s45, s62\n"
      "s_add_u32 s45, s45, 15728640\n"
      "s_waitcnt lgkmcnt(0)\n"
      "s_barrier\n"
      "ds_read_b64_tr_b16 v[88:89], v13 offset:0\n"
      "ds_read_b64_tr_b16 v[90:91], v13 offset:512\n"
      "ds_read_b128 v[80:83], v9\n"
      "ds_read_b128 v[84:87], v9 offset:8192\n"
      "ds_read_b64_tr_b16 v[92:93], v14 offset:0\n"
      "ds_read_b64_tr_b16 v[94:95], v14 offset:512\n"
      "s_branch .Lg1_39_%=\n"
      ".Lloop_38_%=:\n"
      "s_barrier\n"
      "v_mfma_f32_32x32x16_bf16 v[16:31], v[108:111], v[100:103], v[16:31]\n"
      "ds_read_b64_tr_b16 v[88:89], v13 offset:0\n"
      "ds_read_b64_tr_b16 v[90:91], v13 offset:512\n"
      "ds_read_b128 v[80:83], v9\n"
      "v_mfma_f32_32x32x16_bf16 v[48:63], v[108:111], v[104:107], v[48:63]\n"
      "ds_read_b128 v[84:87], v9 offset:8192\n"
      "ds_read_b64_tr_b16 v[92:93], v14 offset:0\n"
      "ds_read_b64_tr_b16 v[94:95], v14 offset:512\n"
      "v_mfma_f32_32x32x16_bf16 v[32:47], v[112:115], v[100:103], v[32:47]\n"
      "v_mfma_f32_32x32x16_bf16 v[64:79], v[112:115], v[104:107], v[64:79]\n"
      ".Lg1_39_%=:\n"
      "s_waitcnt lgkmcnt(3)\n"
      "v_mfma_f32_32x32x16_bf16 v[16:31], v[88:91], v[80:83], v[16:31]\n"
      "ds_read_b64_tr_b16 v[108:109], v13 offset:2048\n"
      "ds_read_b64_tr_b16 v[110:111], v13 offset:2560\n"
      "ds_read_b128 v[100:103], v10\n"
      "s_waitcnt lgkmcnt(5)\n"
      "v_mfma_f32_32x32x16_bf16 v[48:63], v[88:91], v[84:87], v[48:63]\n"
      "ds_read_b128 v[104:107], v10 offset:8192\n"
      "ds_read_b64_tr_b16 v[112:113], v14 offset:2048\n"
      "ds_read_b64_tr_b16 v[114:115], v14 offset:2560\n"
      "s_waitcnt lgkmcnt(6)\n"
      "v_mfma_f32_32x32x16_bf16 v[32:47], v[92:95], v[80:83], v[32:47]\n"
      "v_xor_b32 v11, 64, v9\n"
      "v_xor_b32 v12, 0x60, v9\n"
      "v_mov_b32 v120, v13\n"
      "v_mov_b32 v121, v14\n"
      "v_mfma_f32_32x32x16_bf16 v[64:79], v[92:95], v[84:87], v[64:79]\n"
      "s_add_u32 s52, s52, 32768\n"
      "s_and_b32 s52, s52, 0x1ffff\n"
      "s_add_u32 s56, s56, 1\n"
      "s_cmp_lt_u32 s56, 8\n"
      "s_waitcnt lgkmcnt(3)\n"
      "v_mfma_f32_32x32x16_bf16 v[16:31], v[108:111], v[100:103], v[16:31]\n"
      "ds_read_b64_tr_b16 v[88:89], v120 offset:4096\n"
      "ds_read_b64_tr_b16 v[90:91], v120 offset:4608\n"
      "ds_read_b128 v[80:83], v11\n"
      "s_waitcnt lgkmcnt(5)\n"
      "v_mfma_f32_32x32x16_bf16 v[48:63], v[108:111], v[104:107], v[48:63]\n"
      "ds_read_b128 v[84:87], v11 offset:8192\n"
      "ds_read_b64_tr_b16 v[92:93], v121 offset:4096\n"
      "ds_read_b64_tr_b16 v[94:95], v121 offset:4608\n"
      "s_waitcnt lgkmcnt(6)\n"
      "v_mfma_f32_32x32x16_bf16 v[32:47], v[112:115], v[100:103], v[32:47]\n"
      "v_add_u32 v9, s52, v5\n"
      "v_add_u32 v13, s52, v6\n"
      "v_mfma_f32_32x32x16_bf16 v[64:79], v[112:115], v[104:107], v[64:79]\n"
      "v_add_u32 v14, s52, v7\n"
      "v_xor_b32 v10, 32, v9\n"
      "s_waitcnt lgkmcnt(3)\n"
      "v_mfma_f32_32x32x16_bf16 v[16:31], v[88:91], v[80:83], v[16:31]\n"
      "ds_read_b64_tr_b16 v[108:109], v120 offset:6144\n"
      "ds_read_b64_tr_b16 v[110:111], v120 offset:6656\n"
      "ds_read_b128 v[100:103], v12\n"
      "s_waitcnt lgkmcnt(5)\n"
      "v_mfma_f32_32x32x16_bf16 v[48:63], v[88:91], v[84:87], v[48:63]\n"
      "ds_read_b128 v[104:107], v12 offset:8192\n"
      "ds_read_b64_tr_b16 v[112:113], v121 offset:6144\n"
      "ds_read_b64_tr_b16 v[114:115], v121 offset:6656\n"
      "s_waitcnt lgkmcnt(6)\n"
      "v_mfma_f32_32x32x16_bf16 v[32:47], v[92:95], v[80:83], v[32:47]\n"
      "v_mfma_f32_32x32x16_bf16 v[64:79], v[92:95], v[84:87], v[64:79]\n"
      "s_waitcnt lgkmcnt(0)\n"
      "s_cbranch_scc1 .Lloop_38_%=\n"
      "s_barrier\n"
      "v_mfma_f32_32x32x16_bf16 v[16:31], v[108:111], v[100:103], v[16:31]\n"
      "v_mfma_f32_32x32x16_bf16 v[48:63], v[108:111], v[104:107], v[48:63]\n"
      "v_mfma_f32_32x32x16_bf16 v[32:47], v[112:115], v[100:103], v[32:47]\n"
      "v_mfma_f32_32x32x16_bf16 v[64:79], v[112:115], v[104:107], v[64:79]\n"
      "v_add_u32 v5, 98304, v2\n"
      "v_add_u32 v6, 98304, v3\n"
      "v_mov_b32 v12, s58\n"
      "v_mov_b32 v13, s59\n"
      "v_lshlrev_b32 v7, 10, v0\n"
      "s_setprio 2\n"
      "s_nop 2\n"
      "v_mul_f32 v152, s60, v16\n"
      "v_mul_f32 v153, s60, v17\n"
      "v_mul_f32 v154, s60, v18\n"
      "v_mul_f32 v155, s60, v19\n"
      "v_mul_f32 v156, s60, v20\n"
      "v_mul_f32 v157, s60, v21\n"
      "v_mul_f32 v158, s60, v22\n"
      "v_mul_f32 v159, s60, v23\n"
      "v_mul_f32 v160, s60, v24\n"
      "v_mul_f32 v161, s60, v25\n"
      "v_mul_f32 v162, s60, v26\n"
      "v_mul_f32 v163, s60, v27\n"
      "v_mul_f32 v164, s60, v28\n"
      "v_mul_f32 v165, s60, v29\n"
      "v_mul_f32 v166, s60, v30\n"
      "v_mul_f32 v167, s60, v31\n"
      "v_exp_f32 v152, v152\n"
      "v_exp_f32 v153, v153\n"
      "v_exp_f32 v154, v154\n"
      "v_exp_f32 v155, v155\n"
      "v_exp_f32 v156, v156\n"
      "v_exp_f32 v157, v157\n"
      "v_exp_f32 v158, v158\n"
      "v_exp_f32 v159, v159\n"
      "v_exp_f32 v160, v160\n"
      "v_exp_f32 v161, v161\n"
      "v_exp_f32 v162, v162\n"
      "v_exp_f32 v163, v163\n"
      "v_exp_f32 v164, v164\n"
      "v_exp_f32 v165, v165\n"
      "v_exp_f32 v166, v166\n"
      "v_exp_f32 v167, v167\n"
      "v_fma_f32 v168, v152, 0.5, 1.0\n"
      "v_fma_f32 v169, v153, 0.5, 1.0\n"
      "v_fma_f32 v170, v154, 0.5, 1.0\n"
      "v_fma_f32 v171, v155, 0.5, 1.0\n"
      "v_fma_f32 v172, v156, 0.5, 1.0\n"
      "v_fma_f32 v173, v157, 0.5, 1.0\n"
      "v_fma_f32 v174, v158, 0.5, 1.0\n"
      "v_fma_f32 v175, v159, 0.5, 1.0\n"
      "v_fma_f32 v176, v160, 0.5, 1.0\n"
      "v_fma_f32 v177, v161, 0.5, 1.0\n"
      "v_fma_f32 v178, v162, 0.5, 1.0\n"
      "v_fma_f32 v179, v163, 0.5, 1.0\n"
      "v_fma_f32 v180, v164, 0.5, 1.0\n"
      "v_fma_f32 v181, v165, 0.5, 1.0\n"
      "v_fma_f32 v182, v166, 0.5, 1.0\n"
      "v_fma_f32 v183, v167, 0.5, 1.0\n"
      "v_fma_f32 v152, v152, v168, 1.0\n"
      "v_fma_f32 v153, v153, v169, 1.0\n"
      "v_fma_f32 v154, v154, v170, 1.0\n"
      "v_fma_f32 v155, v155, v171, 1.0\n"
      "v_fma_f32 v156, v156, v172, 1.0\n"
      "v_fma_f32 v157, v157, v173, 1.0\n"
      "v_fma_f32 v158, v158, v174, 1.0\n"
      "v_fma_f32 v159, v159, v175, 1.0\n"
      "v_fma_f32 v160, v160, v176, 1.0\n"
      "v_fma_f32 v161, v161, v177, 1.0\n"
      "v_fma_f32 v162, v162, v178, 1.0\n"
      "v_fma_f32 v163, v163, v179, 1.0\n"
      "v_fma_f32 v164, v164, v180, 1.0\n"
      "v_fma_f32 v165, v165, v181, 1.0\n"
      "v_fma_f32 v166, v166, v182, 1.0\n"
      "v_fma_f32 v167, v167, v183, 1.0\n"
      "v_rcp_f32 v152, v152\n"
      "v_rcp_f32 v153, v153\n"
      "v_rcp_f32 v154, v154\n"
      "v_rcp_f32 v155, v155\n"
      "v_rcp_f32 v156, v156\n"
      "v_rcp_f32 v157, v157\n"
      "v_rcp_f32 v158, v158\n"
      "v_rcp_f32 v159, v159\n"
      "v_rcp_f32 v160, v160\n"
      "v_rcp_f32 v161, v161\n"
      "v_rcp_f32 v162, v162\n"
      "v_rcp_f32 v163, v163\n"
      "v_rcp_f32 v164, v164\n"
      "v_rcp_f32 v165, v165\n"
      "v_rcp_f32 v166, v166\n"
      "v_rcp_f32 v167, v167\n"
      "v_fma_f32 v168, -v16, v152, v16\n"
      "v_fma_f32 v169, -v17, v153, v17\n"
      "v_fma_f32 v170, -v18, v154, v18\n"
      "v_fma_f32 v171, -v19, v155, v19\n"
      "v_fma_f32 v172, -v20, v156, v20\n"
      "v_fma_f32 v173, -v21, v157, v21\n"
      "v_fma_f32 v174, -v22, v158, v22\n"
      "v_fma_f32 v175, -v23, v159, v23\n"
      "v_fma_f32 v176, -v24, v160, v24\n"
      "v_fma_f32 v177, -v25, v161, v25\n"
      "v_fma_f32 v178, -v26, v162, v26\n"
      "v_fma_f32 v179, -v27, v163, v27\n"
      "v_fma_f32 v180, -v28, v164, v28\n"
      "v_fma_f32 v181, -v29, v165, v29\n"
      "v_fma_f32 v182, -v30, v166, v30\n"
      "v_fma_f32 v183, -v31, v167, v31\n"
      "v_cvt_pk_bf16_f32 v216, v168, v169\n"
      "v_cvt_pk_bf16_f32 v217, v170, v171\n"
      "v_cvt_pk_bf16_f32 v218, v172, v173\n"
      "v_cvt_pk_bf16_f32 v219, v174, v175\n"
      "v_cvt_pk_bf16_f32 v220, v176, v177\n"
      "v_cvt_pk_bf16_f32 v221, v178, v179\n"
      "v_cvt_pk_bf16_f32 v222, v180, v181\n"
      "v_cvt_pk_bf16_f32 v223, v182, v183\n"
      "s_setprio 3\n"
      "buffer_store_dwordx4 v[216:219], v1, s[16:19], s40 offen sc1\n"
      "buffer_store_dwordx4 v[220:223], v1, s[16:19], s40 offen offset:1024 sc1\n"
      "s_setprio 2\n"
      "v_mul_f32 v152, s60, v32\n"
      "v_mul_f32 v153, s60, v33\n"
      "v_mul_f32 v154, s60, v34\n"
      "v_mul_f32 v155, s60, v35\n"
      "v_mul_f32 v156, s60, v36\n"
      "v_mul_f32 v157, s60, v37\n"
      "v_mul_f32 v158, s60, v38\n"
      "v_mul_f32 v159, s60, v39\n"
      "v_mul_f32 v160, s60, v40\n"
      "v_mul_f32 v161, s60, v41\n"
      "v_mul_f32 v162, s60, v42\n"
      "v_mul_f32 v163, s60, v43\n"
      "v_mul_f32 v164, s60, v44\n"
      "v_mul_f32 v165, s60, v45\n"
      "v_mul_f32 v166, s60, v46\n"
      "v_mul_f32 v167, s60, v47\n"
      "v_exp_f32 v152, v152\n"
      "v_exp_f32 v153, v153\n"
      "v_exp_f32 v154, v154\n"
      "v_exp_f32 v155, v155\n"
      "v_exp_f32 v156, v156\n"
      "v_exp_f32 v157, v157\n"
      "v_exp_f32 v158, v158\n"
      "v_exp_f32 v159, v159\n"
      "v_exp_f32 v160, v160\n"
      "v_exp_f32 v161, v161\n"
      "v_exp_f32 v162, v162\n"
      "v_exp_f32 v163, v163\n"
      "v_exp_f32 v164, v164\n"
      "v_exp_f32 v165, v165\n"
      "v_exp_f32 v166, v166\n"
      "v_exp_f32 v167, v167\n"
      "v_fma_f32 v168, v152, 0.5, 1.0\n"
      "v_fma_f32 v169, v153, 0.5, 1.0\n"
      "v_fma_f32 v170, v154, 0.5, 1.0\n"
      "v_fma_f32 v171, v155, 0.5, 1.0\n"
      "v_fma_f32 v172, v156, 0.5, 1.0\n"
      "v_fma_f32 v173, v157, 0.5, 1.0\n"
      "v_fma_f32 v174, v158, 0.5, 1.0\n"
      "v_fma_f32 v175, v159, 0.5, 1.0\n"
      "v_fma_f32 v176, v160, 0.5, 1.0\n"
      "v_fma_f32 v177, v161, 0.5, 1.0\n"
      "v_fma_f32 v178, v162, 0.5, 1.0\n"
      "v_fma_f32 v179, v163, 0.5, 1.0\n"
      "v_fma_f32 v180, v164, 0.5, 1.0\n"
      "v_fma_f32 v181, v165, 0.5, 1.0\n"
      "v_fma_f32 v182, v166, 0.5, 1.0\n"
      "v_fma_f32 v183, v167, 0.5, 1.0\n"
      "v_fma_f32 v152, v152, v168, 1.0\n"
      "v_fma_f32 v153, v153, v169, 1.0\n"
      "v_fma_f32 v154, v154, v170, 1.0\n"
      "v_fma_f32 v155, v155, v171, 1.0\n"
      "v_fma_f32 v156, v156, v172, 1.0\n"
      "v_fma_f32 v157, v157, v173, 1.0\n"
      "v_fma_f32 v158, v158, v174, 1.0\n"
      "v_fma_f32 v159, v159, v175, 1.0\n"
      "v_fma_f32 v160, v160, v176, 1.0\n"
      "v_fma_f32 v161, v161, v177, 1.0\n"
      "v_fma_f32 v162, v162, v178, 1.0\n"
      "v_fma_f32 v163, v163, v179, 1.0\n"
      "v_fma_f32 v164, v164, v180, 1.0\n"
      "v_fma_f32 v165, v165, v181, 1.0\n"
      "v_fma_f32 v166, v166, v182, 1.0\n"
      "v_fma_f32 v167, v167, v183, 1.0\n"
      "v_rcp_f32 v152, v152\n"
      "v_rcp_f32 v153, v153\n"
      "v_rcp_f32 v154, v154\n"
      "v_rcp_f32 v155, v155\n"
      "v_rcp_f32 v156, v156\n"
      "v_rcp_f32 v157, v157\n"
      "v_rcp_f32 v158, v158\n"
      "v_rcp_f32 v159, v159\n"
      "v_rcp_f32 v160, v160\n"
      "v_rcp_f32 v161, v161\n"
      "v_rcp_f32 v162, v162\n"
      "v_rcp_f32 v163, v163\n"
      "v_rcp_f32 v164, v164\n"
      "v_rcp_f32 v165, v165\n"
      "v_rcp_f32 v166, v166\n"
      "v_rcp_f32 v167, v167\n"
      "v_fma_f32 v168, -v32, v152, v32\n"
      "v_fma_f32 v169, -v33, v153, v33\n"
      "v_fma_f32 v170, -v34, v154, v34\n"
      "v_fma_f32 v171, -v35, v155, v35\n"
      "v_fma_f32 v172, -v36, v156, v36\n"
      "v_fma_f32 v173, -v37, v157, v37\n"
      "v_fma_f32 v174, -v38, v158, v38\n"
      "v_fma_f32 v175, -v39, v159, v39\n"
      "v_fma_f32 v176, -v40, v160, v40\n"
      "v_fma_f32 v177, -v41, v161, v41\n"
      "v_fma_f32 v178, -v42, v162, v42\n"
      "v_fma_f32 v179, -v43, v163, v43\n"
      "v_fma_f32 v180, -v44, v164, v44\n"
      "v_fma_f32 v181, -v45, v165, v45\n"
      "v_fma_f32 v182, -v46, v166, v46\n"
      "v_fma_f32 v183, -v47, v167, v47\n"
      "v_cvt_pk_bf16_f32 v224, v168, v169\n"
      "v_cvt_pk_bf16_f32 v225, v170, v171\n"
      "v_cvt_pk_bf16_f32 v226, v172, v173\n"
      "v_cvt_pk_bf16_f32 v227, v174, v175\n"
      "v_cvt_pk_bf16_f32 v228, v176, v177\n"
      "v_cvt_pk_bf16_f32 v229, v178, v179\n"
      "v_cvt_pk_bf16_f32 v230, v180, v181\n"
      "v_cvt_pk_bf16_f32 v231, v182, v183\n"
      "s_setprio 3\n"
      "buffer_store_dwordx4 v[224:227], v1, s[16:19], s41 offen sc1\n"
      "buffer_store_dwordx4 v[228:231], v1, s[16:19], s41 offen offset:1024 sc1\n"
      "s_setprio 2\n"
      "v_mul_f32 v152, s60, v48\n"
      "v_mul_f32 v153, s60, v49\n"
      "v_mul_f32 v154, s60, v50\n"
      "v_mul_f32 v155, s60, v51\n"
      "v_mul_f32 v156, s60, v52\n"
      "v_mul_f32 v157, s60, v53\n"
      "v_mul_f32 v158, s60, v54\n"
      "v_mul_f32 v159, s60, v55\n"
      "v_mul_f32 v160, s60, v56\n"
      "v_mul_f32 v161, s60, v57\n"
      "v_mul_f32 v162, s60, v58\n"
      "v_mul_f32 v163, s60, v59\n"
      "v_mul_f32 v164, s60, v60\n"
      "v_mul_f32 v165, s60, v61\n"
      "v_mul_f32 v166, s60, v62\n"
      "v_mul_f32 v167, s60, v63\n"
      "v_exp_f32 v152, v152\n"
      "v_exp_f32 v153, v153\n"
      "v_exp_f32 v154, v154\n"
      "v_exp_f32 v155, v155\n"
      "v_exp_f32 v156, v156\n"
      "v_exp_f32 v157, v157\n"
      "v_exp_f32 v158, v158\n"
      "v_exp_f32 v159, v159\n"
      "v_exp_f32 v160, v160\n"
      "v_exp_f32 v161, v161\n"
      "v_exp_f32 v162, v162\n"
      "v_exp_f32 v163, v163\n"
      "v_exp_f32 v164, v164\n"
      "v_exp_f32 v165, v165\n"
      "v_exp_f32 v166, v166\n"
      "v_exp_f32 v167, v167\n"
      "v_fma_f32 v168, v152, 0.5, 1.0\n"
      "v_fma_f32 v169, v153, 0.5, 1.0\n"
      "v_fma_f32 v170, v154, 0.5, 1.0\n"
      "v_fma_f32 v171, v155, 0.5, 1.0\n"
      "v_fma_f32 v172, v156, 0.5, 1.0\n"
      "v_fma_f32 v173, v157, 0.5, 1.0\n"
      "v_fma_f32 v174, v158, 0.5, 1.0\n"
      "v_fma_f32 v175, v159, 0.5, 1.0\n"
      "v_fma_f32 v176, v160, 0.5, 1.0\n"
      "v_fma_f32 v177, v161, 0.5, 1.0\n"
      "v_fma_f32 v178, v162, 0.5, 1.0\n"
      "v_fma_f32 v179, v163, 0.5, 1.0\n"
      "v_fma_f32 v180, v164, 0.5, 1.0\n"
      "v_fma_f32 v181, v165, 0.5, 1.0\n"
      "v_fma_f32 v182, v166, 0.5, 1.0\n"
      "v_fma_f32 v183, v167, 0.5, 1.0\n"
      "v_fma_f32 v152, v152, v168, 1.0\n"
      "v_fma_f32 v153, v153, v169, 1.0\n"
      "v_fma_f32 v154, v154, v170, 1.0\n"
      "v_fma_f32 v155, v155, v171, 1.0\n"
      "v_fma_f32 v156, v156, v172, 1.0\n"
      "v_fma_f32 v157, v157, v173, 1.0\n"
      "v_fma_f32 v158, v158, v174, 1.0\n"
      "v_fma_f32 v159, v159, v175, 1.0\n"
      "v_fma_f32 v160, v160, v176, 1.0\n"
      "v_fma_f32 v161, v161, v177, 1.0\n"
      "v_fma_f32 v162, v162, v178, 1.0\n"
      "v_fma_f32 v163, v163, v179, 1.0\n"
      "v_fma_f32 v164, v164, v180, 1.0\n"
      "v_fma_f32 v165, v165, v181, 1.0\n"
      "v_fma_f32 v166, v166, v182, 1.0\n"
      "v_fma_f32 v167, v167, v183, 1.0\n"
      "v_rcp_f32 v152, v152\n"
      "v_rcp_f32 v153, v153\n"
      "v_rcp_f32 v154, v154\n"
      "v_rcp_f32 v155, v155\n"
      "v_rcp_f32 v156, v156\n"
      "v_rcp_f32 v157, v157\n"
      "v_rcp_f32 v158, v158\n"
      "v_rcp_f32 v159, v159\n"
      "v_rcp_f32 v160, v160\n"
      "v_rcp_f32 v161, v161\n"
      "v_rcp_f32 v162, v162\n"
      "v_rcp_f32 v163, v163\n"
      "v_rcp_f32 v164, v164\n"
      "v_rcp_f32 v165, v165\n"
      "v_rcp_f32 v166, v166\n"
      "v_rcp_f32 v167, v167\n"
      "v_fma_f32 v168, -v48, v152, v48\n"
      "v_fma_f32 v169, -v49, v153, v49\n"
      "v_fma_f32 v170, -v50, v154, v50\n"
      "v_fma_f32 v171, -v51, v155, v51\n"
      "v_fma_f32 v172, -v52, v156, v52\n"
      "v_fma_f32 v173, -v53, v157, v53\n"
      "v_fma_f32 v174, -v54, v158, v54\n"
      "v_fma_f32 v175, -v55, v159, v55\n"
      "v_fma_f32 v176, -v56, v160, v56\n"
      "v_fma_f32 v177, -v57, v161, v57\n"
      "v_fma_f32 v178, -v58, v162, v58\n"
      "v_fma_f32 v179, -v59, v163, v59\n"
      "v_fma_f32 v180, -v60, v164, v60\n"
      "v_fma_f32 v181, -v61, v165, v61\n"
      "v_fma_f32 v182, -v62, v166, v62\n"
      "v_fma_f32 v183, -v63, v167, v63\n"
      "v_cvt_pk_bf16_f32 v216, v168, v169\n"
      "v_cvt_pk_bf16_f32 v217, v170, v171\n"
      "v_cvt_pk_bf16_f32 v218, v172, v173\n"
      "v_cvt_pk_bf16_f32 v219, v174, v175\n"
      "v_cvt_pk_bf16_f32 v220, v176, v177\n"
      "v_cvt_pk_bf16_f32 v221, v178, v179\n"
      "v_cvt_pk_bf16_f32 v222, v180, v181\n"
      "v_cvt_pk_bf16_f32 v223, v182, v183\n"
      "s_setprio 3\n"
      "s_waitcnt vmcnt(0)\n"
      "s_mov_b64 exec, 0xff\n"
      "buffer_atomic_swap_x2 v[12:13], v7, s[16:19], s45 offen sc1\n"
      "s_mov_b64 exec, -1\n"
      "s_add_u32 s45, s45, 262144\n"
      "buffer_store_dwordx4 v[216:219], v1, s[16:19], s42 offen sc1\n"
      "buffer_store_dwordx4 v[220:223], v1, s[16:19], s42 offen offset:1024 sc1\n"
      "s_setprio 2\n"
      "v_mul_f32 v152, s60, v64\n"
      "v_mul_f32 v153, s60, v65\n"
      "v_mul_f32 v154, s60, v66\n"
      "v_mul_f32 v155, s60, v67\n"
      "v_mul_f32 v156, s60, v68\n"
      "v_mul_f32 v157, s60, v69\n"
      "v_mul_f32 v158, s60, v70\n"
      "v_mul_f32 v159, s60, v71\n"
      "v_mul_f32 v160, s60, v72\n"
      "v_mul_f32 v161, s60, v73\n"
      "v_mul_f32 v162, s60, v74\n"
      "v_mul_f32 v163, s60, v75\n"
      "v_mul_f32 v164, s60, v76\n"
      "v_mul_f32 v165, s60, v77\n"
      "v_mul_f32 v166, s60, v78\n"
      "v_mul_f32 v167, s60, v79\n"
      "v_exp_f32 v152, v152\n"
      "v_exp_f32 v153, v153\n"
      "v_exp_f32 v154, v154\n"
      "v_exp_f32 v155, v155\n"
      "v_exp_f32 v156, v156\n"
      "v_exp_f32 v157, v157\n"
      "v_exp_f32 v158, v158\n"
      "v_exp_f32 v159, v159\n"
      "v_exp_f32 v160, v160\n"
      "v_exp_f32 v161, v161\n"
      "v_exp_f32 v162, v162\n"
      "v_exp_f32 v163, v163\n"
      "v_exp_f32 v164, v164\n"
      "v_exp_f32 v165, v165\n"
      "v_exp_f32 v166, v166\n"
      "v_exp_f32 v167, v167\n"
      "v_fma_f32 v168, v152, 0.5, 1.0\n"
      "v_fma_f32 v169, v153, 0.5, 1.0\n"
      "v_fma_f32 v170, v154, 0.5, 1.0\n"
      "v_fma_f32 v171, v155, 0.5, 1.0\n"
      "v_fma_f32 v172, v156, 0.5, 1.0\n"
      "v_fma_f32 v173, v157, 0.5, 1.0\n"
      "v_fma_f32 v174, v158, 0.5, 1.0\n"
      "v_fma_f32 v175, v159, 0.5, 1.0\n"
      "s_setprio 3\n"
      "s_mov_b64 exec, 15\n"
      "v_mov_b64 v[254:255], 0\n"
      "buffer_atomic_or_x2 v[254:255], v252, s[16:19], s54 offen sc0 sc1\n"
      "s_mov_b64 exec, -1\n"
      "s_setprio 2\n"
      "v_fma_f32 v176, v160, 0.5, 1.0\n"
      "v_fma_f32 v177, v161, 0.5, 1.0\n"
      "v_fma_f32 v178, v162, 0.5, 1.0\n"
      "v_fma_f32 v179, v163, 0.5, 1.0\n"
      "v_fma_f32 v180, v164, 0.5, 1.0\n"
      "v_fma_f32 v181, v165, 0.5, 1.0\n"
      "v_fma_f32 v182, v166, 0.5, 1.0\n"
      "v_fma_f32 v183, v167, 0.5, 1.0\n"
      "v_fma_f32 v152, v152, v168, 1.0\n"
      "v_fma_f32 v153, v153, v169, 1.0\n"
      "v_fma_f32 v154, v154, v170, 1.0\n"
      "v_fma_f32 v155, v155, v171, 1.0\n"
      "v_fma_f32 v156, v156, v172, 1.0\n"
      "v_fma_f32 v157, v157, v173, 1.0\n"
      "v_fma_f32 v158, v158, v174, 1.0\n"
      "v_fma_f32 v159, v159, v175, 1.0\n"
      "v_fma_f32 v160, v160, v176, 1.0\n"
      "v_fma_f32 v161, v161, v177, 1.0\n"
      "v_fma_f32 v162, v162, v178, 1.0\n"
      "v_fma_f32 v163, v163, v179, 1.0\n"
      "v_fma_f32 v164, v164, v180, 1.0\n"
      "v_fma_f32 v165, v165, v181, 1.0\n"
      "v_fma_f32 v166, v166, v182, 1.0\n"
      "v_fma_f32 v167, v167, v183, 1.0\n"
      "v_rcp_f32 v152, v152\n"
      "v_rcp_f32 v153, v153\n"
      "v_rcp_f32 v154, v154\n"
      "v_rcp_f32 v155, v155\n"
      "v_rcp_f32 v156, v156\n"
      "v_rcp_f32 v157, v157\n"
      "v_rcp_f32 v158, v158\n"
      "v_rcp_f32 v159, v159\n"
      "v_rcp_f32 v160, v160\n"
      "v_rcp_f32 v161, v161\n"
      "v_rcp_f32 v162, v162\n"
      "v_rcp_f32 v163, v163\n"
      "v_rcp_f32 v164, v164\n"
      "v_rcp_f32 v165, v165\n"
      "v_rcp_f32 v166, v166\n"
      "v_rcp_f32 v167, v167\n"
      "v_fma_f32 v168, -v64, v152, v64\n"
      "v_fma_f32 v169, -v65, v153, v65\n"
      "v_fma_f32 v170, -v66, v154, v66\n"
      "v_fma_f32 v171, -v67, v155, v67\n"
      "v_fma_f32 v172, -v68, v156, v68\n"
      "v_fma_f32 v173, -v69, v157, v69\n"
      "v_fma_f32 v174, -v70, v158, v70\n"
      "v_fma_f32 v175, -v71, v159, v71\n"
      "v_fma_f32 v176, -v72, v160, v72\n"
      "v_fma_f32 v177, -v73, v161, v73\n"
      "v_fma_f32 v178, -v74, v162, v74\n"
      "v_fma_f32 v179, -v75, v163, v75\n"
      "v_fma_f32 v180, -v76, v164, v76\n"
      "v_fma_f32 v181, -v77, v165, v77\n"
      "v_fma_f32 v182, -v78, v166, v78\n"
      "v_fma_f32 v183, -v79, v167, v79\n"
      "v_cvt_pk_bf16_f32 v224, v168, v169\n"
      "v_cvt_pk_bf16_f32 v225, v170, v171\n"
      "v_cvt_pk_bf16_f32 v226, v172, v173\n"
      "v_cvt_pk_bf16_f32 v227, v174, v175\n"
      "v_cvt_pk_bf16_f32 v228, v176, v177\n"
      "v_cvt_pk_bf16_f32 v229, v178, v179\n"
      "v_cvt_pk_bf16_f32 v230, v180, v181\n"
      "v_cvt_pk_bf16_f32 v231, v182, v183\n"
      "s_setprio 3\n"
      "buffer_store_dwordx4 v[224:227], v1, s[16:19], s43 offen sc1\n"
      "buffer_store_dwordx4 v[228:231], v1, s[16:19], s43 offen offset:1024 sc1\n"
      "s_mov_b32 s53, 1\n"
      "s_waitcnt vmcnt(2)\n"
      "s_mov_b64 exec, 15\n"
      "v_cmp_ne_u64 vcc, s[58:59], v[254:255]\n"
      "s_mov_b64 exec, -1\n"
      "s_cbranch_vccz .LPA2_5_%=\n"
      "s_mov_b32 s53, 0\n"
      "s_waitcnt vmcnt(0)\n"
      "s_mov_b64 exec, 0xff\n"
      "buffer_atomic_swap_x2 v[12:13], v7, s[16:19], s45 offen sc1\n"
      "s_mov_b64 exec, -1\n"
      "s_branch .LPA_3_%=\n"
      ".LQA_6_%=:\n"
      "s_mov_b32 s68, 0x358637bd\n"
      "s_mov_b32 s69, 0x358637bd\n"
      "v_mov_b32 v248, 0x3f803f80\n"
      "v_mov_b32 v249, 0x3f803f80\n"
      "v_mov_b32 v250, 0x3f803f80\n"
      "v_mov_b32 v251, 0x3f803f80\n"
      "s_and_b32 s62, s32, 1\n"
      "s_lshl1_add_u32 s62, s35, s62\n"
      "s_lshr_b32 s63, s32, 1\n"
      "s_lshl1_add_u32 s62, s62, s63\n"
      "s_lshl_b32 s45, s62, 11\n"
      "s_lshl3_add_u32 s45, s34, s45\n"
      "s_add_u32 s45, s45, 4096\n"
      "s_lshl4_add_u32 s62, s62, s34\n"
      "s_lshl_b32 s41, s62, 10\n"
      "s_add_u32 s41, s41, 10485760\n"
      "s_lshl_b32 s42, s62, 6\n"
      "s_add_u32 s42, s42, 11010048\n"
      "s_mov_b32 s53, 0\n"
      "s_waitcnt lgkmcnt(0)\n"
      "s_barrier\n"
      "ds_read_b64_tr_b16 v[88:89], v13 offset:0\n"
      "ds_read_b64_tr_b16 v[90:91], v13 offset:512\n"
      "ds_read_b128 v[80:83], v9\n"
      "ds_read_b128 v[84:87], v9 offset:8192\n"
      "ds_read_b64_tr_b16 v[92:93], v14 offset:0\n"
      "ds_read_b64_tr_b16 v[94:95], v14 offset:512\n"
      "s_branch .Lg1_41_%=\n"
      ".Lloop_40_%=:\n"
      "s_barrier\n"
      "v_mfma_f32_32x32x16_bf16 v[16:31], v[100:103], v[108:111], v[16:31]\n"
      "ds_read_b64_tr_b16 v[88:89], v13 offset:0\n"
      "ds_read_b64_tr_b16 v[90:91], v13 offset:512\n"
      "ds_read_b128 v[80:83], v9\n"
      "v_mfma_f32_32x32x16_bf16 v[32:47], v[104:107], v[108:111], v[32:47]\n"
      "ds_read_b128 v[84:87], v9 offset:8192\n"
      "ds_read_b64_tr_b16 v[92:93], v14 offset:0\n"
      "ds_read_b64_tr_b16 v[94:95], v14 offset:512\n"
      "v_mfma_f32_32x32x16_bf16 v[48:63], v[100:103], v[112:115], v[48:63]\n"
      "v_mfma_f32_32x32x16_bf16 v[64:79], v[104:107], v[112:115], v[64:79]\n"
      ".Lg1_41_%=:\n"
      "s_waitcnt lgkmcnt(3)\n"
      "v_mfma_f32_32x32x16_bf16 v[16:31], v[80:83], v[88:91], v[16:31]\n"
      "ds_read_b64_tr_b16 v[108:109], v13 offset:2048\n"
      "ds_read_b64_tr_b16 v[110:111], v13 offset:2560\n"
      "ds_read_b128 v[100:103], v10\n"
      "s_waitcnt lgkmcnt(5)\n"
      "v_mfma_f32_32x32x16_bf16 v[32:47], v[84:87], v[88:91], v[32:47]\n"
      "ds_read_b128 v[104:107], v10 offset:8192\n"
      "ds_read_b64_tr_b16 v[112:113], v14 offset:2048\n"
      "ds_read_b64_tr_b16 v[114:115], v14 offset:2560\n"
      "s_waitcnt lgkmcnt(6)\n"
      "v_mfma_f32_32x32x16_bf16 v[48:63], v[80:83], v[92:95], v[48:63]\n"
      "v_xor_b32 v11, 64, v9\n"
      "v_xor_b32 v12, 0x60, v9\n"
      "v_mov_b32 v120, v13\n"
      "v_mov_b32 v121, v14\n"
      "v_mfma_f32_32x32x16_bf16 v[64:79], v[84:87], v[92:95], v[64:79]\n"
      "s_add_u32 s52, s52, 32768\n"
      "s_and_b32 s52, s52, 0x1ffff\n"
      "s_add_u32 s56, s56, 1\n"
      "s_cmp_lt_u32 s56, 8\n"
      "s_waitcnt lgkmcnt(3)\n"
      "v_mfma_f32_32x32x16_bf16 v[16:31], v[100:103], v[108:111], v[16:31]\n"
      "ds_read_b64_tr_b16 v[88:89], v120 offset:4096\n"
      "ds_read_b64_tr_b16 v[90:91], v120 offset:4608\n"
      "ds_read_b128 v[80:83], v11\n"
      "s_waitcnt lgkmcnt(5)\n"
      "v_mfma_f32_32x32x16_bf16 v[32:47], v[104:107], v[108:111], v[32:47]\n"
      "ds_read_b128 v[84:87], v11 offset:8192\n"
      "ds_read_b64_tr_b16 v[92:93], v121 offset:4096\n"
      "ds_read_b64_tr_b16 v[94:95], v121 offset:4608\n"
      "s_waitcnt lgkmcnt(6)\n"
      "v_mfma_f32_32x32x16_bf16 v[48:63], v[100:103], v[112:115], v[48:63]\n"
      "v_add_u32 v9, s52, v5\n"
      "v_add_u32 v13, s52, v6\n"
      "v_mfma_f32_32x32x16_bf16 v[64:79], v[104:107], v[112:115], v[64:79]\n"
      "v_add_u32 v14, s52, v7\n"
      "v_xor_b32 v10, 32, v9\n"
      "s_waitcnt lgkmcnt(3)\n"
      "v_mfma_f32_32x32x16_bf16 v[16:31], v[80:83], v[88:91], v[16:31]\n"
      "ds_read_b64_tr_b16 v[108:109], v120 offset:6144\n"
      "ds_read_b64_tr_b16 v[110:111], v120 offset:6656\n"
      "ds_read_b128 v[100:103], v12\n"
      "s_waitcnt lgkmcnt(5)\n"
      "v_mfma_f32_32x32x16_bf16 v[32:47], v[84:87], v[88:91], v[32:47]\n"
      "ds_read_b128 v[104:107], v12 offset:8192\n"
      "ds_read_b64_tr_b16 v[112:113], v121 offset:6144\n"
      "ds_read_b64_tr_b16 v[114:115], v121 offset:6656\n"
      "s_waitcnt lgkmcnt(6)\n"
      "v_mfma_f32_32x32x16_bf16 v[48:63], v[80:83], v[92:95], v[48:63]\n"
      "v_mfma_f32_32x32x16_bf16 v[64:79], v[84:87], v[92:95], v[64:79]\n"
      "s_waitcnt lgkmcnt(0)\n"
      "s_cbranch_scc1 .Lloop_40_%=\n"
      "s_barrier\n"
      "v_mfma_f32_32x32x16_bf16 v[16:31], v[100:103], v[108:111], v[16:31]\n"
      "v_mfma_f32_32x32x16_bf16 v[32:47], v[104:107], v[108:111], v[32:47]\n"
      "v_mfma_f32_32x32x16_bf16 v[48:63], v[100:103], v[112:115], v[48:63]\n"
      "v_mfma_f32_32x32x16_bf16 v[64:79], v[104:107], v[112:115], v[64:79]\n"
      "v_add_u32 v5, 98304, v2\n"
      "v_add_u32 v6, 98304, v3\n"
      "v_mov_b32 v12, s58\n"
      "v_mov_b32 v13, s59\n"
      "v_lshlrev_b32 v7, 7, v0\n"
      "s_setprio 3\n"
      "s_nop 2\n"
      "v_mul_f32 v152, s60, v16\n"
      "v_mul_f32 v153, s60, v17\n"
      "v_mul_f32 v154, s60, v18\n"
      "v_mul_f32 v155, s60, v19\n"
      "v_mul_f32 v156, s60, v20\n"
      "v_mul_f32 v157, s60, v21\n"
      "v_mul_f32 v158, s60, v22\n"
      "v_mul_f32 v159, s60, v23\n"
      "v_mul_f32 v160, s60, v24\n"
      "v_mul_f32 v161, s60, v25\n"
      "v_mul_f32 v162, s60, v26\n"
      "v_mul_f32 v163, s60, v27\n"
      "v_mul_f32 v164, s60, v28\n"
      "v_mul_f32 v165, s60, v29\n"
      "v_mul_f32 v166, s60, v30\n"
      "v_mul_f32 v167, s60, v31\n"
      "v_exp_f32_e64 v152, v152 clamp\n"
      "v_exp_f32_e64 v153, v153 clamp\n"
      "v_exp_f32_e64 v154, v154 clamp\n"
      "v_exp_f32_e64 v155, v155 clamp\n"
      "v_exp_f32_e64 v156, v156 clamp\n"
      "v_exp_f32_e64 v157, v157 clamp\n"
      "v_exp_f32_e64 v158, v158 clamp\n"
      "v_exp_f32_e64 v159, v159 clamp\n"
      "v_exp_f32_e64 v160, v160 clamp\n"
      "v_exp_f32_e64 v161, v161 clamp\n"
      "v_exp_f32_e64 v162, v162 clamp\n"
      "v_exp_f32_e64 v163, v163 clamp\n"
      "v_exp_f32_e64 v164, v164 clamp\n"
      "v_exp_f32_e64 v165, v165 clamp\n"
      "v_exp_f32_e64 v166, v166 clamp\n"
      "v_exp_f32_e64 v167, v167 clamp\n"
      "v_max_f32 v16, 0, v16\n"
      "v_max_f32 v17, 0, v17\n"
      "v_max_f32 v18, 0, v18\n"
      "v_max_f32 v19, 0, v19\n"
      "v_max_f32 v20, 0, v20\n"
      "v_max_f32 v21, 0, v21\n"
      "v_max_f32 v22, 0, v22\n"
      "v_max_f32 v23, 0, v23\n"
      "v_max_f32 v24, 0, v24\n"
      "v_max_f32 v25, 0, v25\n"
      "v_max_f32 v26, 0, v26\n"
      "v_max_f32 v27, 0, v27\n"
      "v_max_f32 v28, 0, v28\n"
      "v_max_f32 v29, 0, v29\n"
      "v_max_f32 v30, 0, v30\n"
      "v_max_f32 v31, 0, v31\n"
      "v_add_f32 v152, v152, v16\n"
      "v_add_f32 v153, v153, v17\n"
      "v_add_f32 v154, v154, v18\n"
      "v_add_f32 v155, v155, v19\n"
      "v_add_f32 v156, v156, v20\n"
      "v_add_f32 v157, v157, v21\n"
      "v_add_f32 v158, v158, v22\n"
      "v_add_f32 v159, v159, v23\n"
      "v_add_f32 v160, v160, v24\n"
      "v_add_f32 v161, v161, v25\n"
      "v_add_f32 v162, v162, v26\n"
      "v_add_f32 v163, v163, v27\n"
      "v_add_f32 v164, v164, v28\n"
      "v_add_f32 v165, v165, v29\n"
      "v_add_f32 v166, v166, v30\n"
      "v_add_f32 v167, v167, v31\n"
      "v_add_f32 v152, s68, v152\n"
      "v_add_f32 v153, s68, v153\n"
      "v_add_f32 v154, s68, v154\n"
      "v_add_f32 v155, s68, v155\n"
      "v_add_f32 v156, s68, v156\n"
      "v_add_f32 v157, s68, v157\n"
      "v_add_f32 v158, s68, v158\n"
      "v_add_f32 v159, s68, v159\n"
      "v_add_f32 v160, s68, v160\n"
      "v_add_f32 v161, s68, v161\n"
      "v_add_f32 v162, s68, v162\n"
      "v_add_f32 v163, s68, v163\n"
      "v_add_f32 v164, s68, v164\n"
      "v_add_f32 v165, s68, v165\n"
      "v_add_f32 v166, s68, v166\n"
      "v_add_f32 v167, s68, v167\n"
      "v_cvt_pk_bf16_f32 v216, v152, v153\n"
      "v_cvt_pk_bf16_f32 v217, v154, v155\n"
      "v_cvt_pk_bf16_f32 v218, v156, v157\n"
      "v_cvt_pk_bf16_f32 v219, v158, v159\n"
      "v_cvt_pk_bf16_f32 v220, v160, v161\n"
      "v_cvt_pk_bf16_f32 v221, v162, v163\n"
      "v_cvt_pk_bf16_f32 v222, v164, v165\n"
      "v_cvt_pk_bf16_f32 v223, v166, v167\n"
      "v_cvt_pk_bf16_f32 v232, v48, v49\n"
      "v_cvt_pk_bf16_f32 v233, v50, v51\n"
      "v_cvt_pk_bf16_f32 v234, v52, v53\n"
      "v_cvt_pk_bf16_f32 v235, v54, v55\n"
      "v_cvt_pk_bf16_f32 v236, v56, v57\n"
      "v_cvt_pk_bf16_f32 v237, v58, v59\n"
      "v_cvt_pk_bf16_f32 v238, v60, v61\n"
      "v_cvt_pk_bf16_f32 v239, v62, v63\n"
      "v_mfma_f32_32x32x16_bf16 v[184:199], v[216:219], v[232:235], 0\n"
      "v_mfma_f32_32x32x16_bf16 v[200:215], v[216:219], v[248:251], 0\n"
      "v_mfma_f32_32x32x16_bf16 v[184:199], v[220:223], v[236:239], v[184:199]\n"
      "v_mfma_f32_32x32x16_bf16 v[200:215], v[220:223], v[248:251], v[200:215]\n"
      "v_mul_f32 v152, s60, v32\n"
      "v_mul_f32 v153, s60, v33\n"
      "v_mul_f32 v154, s60, v34\n"
      "v_mul_f32 v155, s60, v35\n"
      "v_mul_f32 v156, s60, v36\n"
      "v_mul_f32 v157, s60, v37\n"
      "v_mul_f32 v158, s60, v38\n"
      "v_mul_f32 v159, s60, v39\n"
      "v_mul_f32 v160, s60, v40\n"
      "v_mul_f32 v161, s60, v41\n"
      "v_mul_f32 v162, s60, v42\n"
      "v_mul_f32 v163, s60, v43\n"
      "v_mul_f32 v164, s60, v44\n"
      "v_mul_f32 v165, s60, v45\n"
      "v_mul_f32 v166, s60, v46\n"
      "v_mul_f32 v167, s60, v47\n"
      "v_exp_f32_e64 v152, v152 clamp\n"
      "v_exp_f32_e64 v153, v153 clamp\n"
      "v_exp_f32_e64 v154, v154 clamp\n"
      "v_exp_f32_e64 v155, v155 clamp\n"
      "v_exp_f32_e64 v156, v156 clamp\n"
      "v_exp_f32_e64 v157, v157 clamp\n"
      "v_exp_f32_e64 v158, v158 clamp\n"
      "v_exp_f32_e64 v159, v159 clamp\n"
      "v_exp_f32_e64 v160, v160 clamp\n"
      "v_exp_f32_e64 v161, v161 clamp\n"
      "v_exp_f32_e64 v162, v162 clamp\n"
      "v_exp_f32_e64 v163, v163 clamp\n"
      "v_exp_f32_e64 v164, v164 clamp\n"
      "v_exp_f32_e64 v165, v165 clamp\n"
      "v_exp_f32_e64 v166, v166 clamp\n"
      "v_exp_f32_e64 v167, v167 clamp\n"
      "v_max_f32 v32, 0, v32\n"
      "v_max_f32 v33, 0, v33\n"
      "v_max_f32 v34, 0, v34\n"
      "v_max_f32 v35, 0, v35\n"
      "v_max_f32 v36, 0, v36\n"
      "v_max_f32 v37, 0, v37\n"
      "v_max_f32 v38, 0, v38\n"
      "v_max_f32 v39, 0, v39\n"
      "v_max_f32 v40, 0, v40\n"
      "v_max_f32 v41, 0, v41\n"
      "v_max_f32 v42, 0, v42\n"
      "v_max_f32 v43, 0, v43\n"
      "v_max_f32 v44, 0, v44\n"
      "v_max_f32 v45, 0, v45\n"
      "v_max_f32 v46, 0, v46\n"
      "v_max_f32 v47, 0, v47\n"
      "v_add_f32 v152, v152, v32\n"
      "v_add_f32 v153, v153, v33\n"
      "v_add_f32 v154, v154, v34\n"
      "v_add_f32 v155, v155, v35\n"
      "v_add_f32 v156, v156, v36\n"
      "v_add_f32 v157, v157, v37\n"
      "v_add_f32 v158, v158, v38\n"
      "v_add_f32 v159, v159, v39\n"
      "v_add_f32 v160, v160, v40\n"
      "v_add_f32 v161, v161, v41\n"
      "v_add_f32 v162, v162, v42\n"
      "v_add_f32 v163, v163, v43\n"
      "v_add_f32 v164, v164, v44\n"
      "v_add_f32 v165, v165, v45\n"
      "v_add_f32 v166, v166, v46\n"
      "v_add_f32 v167, v167, v47\n"
      "v_add_f32 v152, s68, v152\n"
      "v_add_f32 v153, s68, v153\n"
      "v_add_f32 v154, s68, v154\n"
      "v_add_f32 v155, s68, v155\n"
      "v_add_f32 v156, s68, v156\n"
      "v_add_f32 v157, s68, v157\n"
      "v_add_f32 v158, s68, v158\n"
      "v_add_f32 v159, s68, v159\n"
      "v_add_f32 v160, s68, v160\n"
      "v_add_f32 v161, s68, v161\n"
      "v_add_f32 v162, s68, v162\n"
      "v_add_f32 v163, s68, v163\n"
      "v_add_f32 v164, s68, v164\n"
      "v_add_f32 v165, s68, v165\n"
      "v_add_f32 v166, s68, v166\n"
      "v_add_f32 v167, s68, v167\n"
      "v_cvt_pk_bf16_f32 v224, v152, v153\n"
      "v_cvt_pk_bf16_f32 v225, v154, v155\n"
      "v_cvt_pk_bf16_f32 v226, v156, v157\n"
      "v_cvt_pk_bf16_f32 v227, v158, v159\n"
      "v_cvt_pk_bf16_f32 v228, v160, v161\n"
      "v_cvt_pk_bf16_f32 v229, v162, v163\n"
      "v_cvt_pk_bf16_f32 v230, v164, v165\n"
      "v_cvt_pk_bf16_f32 v231, v166, v167\n"
      "v_cvt_pk_bf16_f32 v240, v64, v65\n"
      "v_cvt_pk_bf16_f32 v241, v66, v67\n"
      "v_cvt_pk_bf16_f32 v242, v68, v69\n"
      "v_cvt_pk_bf16_f32 v243, v70, v71\n"
      "v_cvt_pk_bf16_f32 v244, v72, v73\n"
      "v_cvt_pk_bf16_f32 v245, v74, v75\n"
      "v_cvt_pk_bf16_f32 v246, v76, v77\n"
      "v_cvt_pk_bf16_f32 v247, v78, v79\n"
      "v_mfma_f32_32x32x16_bf16 v[184:199], v[224:227], v[240:243], v[184:199]\n"
      "v_mfma_f32_32x32x16_bf16 v[200:215], v[224:227], v[248:251], v[200:215]\n"
      "v_mfma_f32_32x32x16_bf16 v[184:199], v[228:231], v[244:247], v[184:199]\n"
      "v_mfma_f32_32x32x16_bf16 v[200:215], v[228:231], v[248:251], v[200:215]\n"
      "v_and_b32 v9, 16, v0\n"
      "v_cmp_ne_u32 vcc, 0, v9\n"
      "s_lshl_b32 s64, s32, 11\n"
      "s_add_u32 s64, s64, 148480\n"
      "v_add_u32 v9, s64, v1\n"
      "s_xor_b32 s65, s32, 2\n"
      "s_lshl_b32 s64, s65, 11\n"
      "s_add_u32 s64, s64, 148480\n"
      "v_add_u32 v10, s64, v1\n"
      "s_lshl_b32 s64, s32, 2\n"
      "s_add_u32 s64, s64, 160768\n"
      "v_mov_b32 v11, s64\n"
      "v_xor_b32 v14, 8, v11\n"
      "v_cndmask_b32 v152, v184, v192, vcc\n"
      "v_cndmask_b32 v153, v185, v193, vcc\n"
      "v_cndmask_b32 v154, v186, v194, vcc\n"
      "v_cndmask_b32 v155, v187, v195, vcc\n"
      "v_cndmask_b32 v156, v188, v196, vcc\n"
      "v_cndmask_b32 v157, v189, v197, vcc\n"
      "v_cndmask_b32 v158, v190, v198, vcc\n"
      "v_cndmask_b32 v159, v191, v199, vcc\n"
      "v_cndmask_b32 v160, v200, v208, vcc\n"
      "v_cndmask_b32 v161, v201, v209, vcc\n"
      "v_cndmask_b32 v162, v202, v210, vcc\n"
      "v_cndmask_b32 v163, v203, v211, vcc\n"
      "v_cndmask_b32 v164, v204, v212, vcc\n"
      "v_cndmask_b32 v165, v205, v213, vcc\n"
      "v_cndmask_b32 v166, v206, v214, vcc\n"
      "v_cndmask_b32 v167, v207, v215, vcc\n"
      "s_cmp_eq_u32 s63, 0\n"
      "s_cbranch_scc1 .Lth_42_%=\n"
      "v_swap_b32 v152, v156\n"
      "v_swap_b32 v160, v164\n"
      "v_swap_b32 v153, v157\n"
      "v_swap_b32 v161, v165\n"
      "v_swap_b32 v154, v158\n"
      "v_swap_b32 v162, v166\n"
      "v_swap_b32 v155, v159\n"
      "v_swap_b32 v163, v167\n"
      ".Lth_42_%=:\n"
      "ds_write_b128 v9, v[156:159]\n"
      "ds_write_b128 v9, v[164:167] offset:1024\n"
      "v_mov_b32 v15, 1\n"
      "ds_write_b32 v11, v15\n"
      "s_mov_b32 s76, 0x40000\n"
      ".Lpw_43_%=:\n"
      "ds_read_b32 v15, v14\n"
      "ds_read_b128 v[168:171], v10\n"
      "ds_read_b128 v[172:175], v10 offset:1024\n"
      "s_waitcnt lgkmcnt(0)\n"
      "s_sub_u32 s76, s76, 1\n"
      "s_cbranch_scc1 .Lpwx_44_%=\n"
      "v_cmp_eq_u32 vcc, 0, v15\n"
      "s_cbranch_vccnz .Lpw_43_%=\n"
      ".Lpwx_44_%=:\n"
      "v_add_f32 v152, v152, v168\n"
      "v_add_f32 v160, v160, v172\n"
      "v_add_f32 v153, v153, v169\n"
      "v_add_f32 v161, v161, v173\n"
      "v_add_f32 v154, v154, v170\n"
      "v_add_f32 v162, v162, v174\n"
      "v_add_f32 v155, v155, v171\n"
      "v_add_f32 v163, v163, v175\n"
      "v_and_b32 v9, 48, v0\n"
      "buffer_store_dwordx4 v[152:155], v1, s[16:19], s41 offen sc1\n"
      "s_mov_b32 exec_lo, 0x00010001\n"
      "s_mov_b32 exec_hi, 0x00010001\n"
      "buffer_store_dwordx4 v[160:163], v9, s[16:19], s42 offen sc1\n"
      "s_mov_b64 exec, -1\n"
      "s_waitcnt vmcnt(0)\n"
      "s_mov_b64 exec, 0xffff\n"
      "buffer_atomic_swap_x2 v[12:13], v7, s[16:19], s45 offen sc1\n"
      "s_mov_b64 exec, -1\n"
      "s_setprio 0\n"
      "s_setprio 3\n"
      "s_and_b32 s62, s99, 1\n"
      "s_lshl1_add_u32 s62, s35, s62\n"
      "s_lshr_b32 s63, s99, 1\n"
      "s_lshl1_add_u32 s62, s62, s63\n"
      "s_lshl4_add_u32 s66, s62, s34\n"
      "s_lshl_b32 s66, s66, 7\n"
      "s_add_u32 s66, s66, 4096\n"
      "s_mov_b64 exec, 65535\n"
      "v_mov_b64 v[248:249], 0\n"
      "buffer_atomic_or_x2 v[248:249], v252, s[16:19], s66 offen sc0 sc1\n"
      "s_mov_b64 exec, -1\n"
      "s_lshl_b32 s67, s62, 14\n"
      "s_add_u32 s67, s67, 10485760\n"
      "s_lshl_b32 s65, s62, 10\n"
      "s_add_u32 s65, s65, 11010048\n"
      "v_and_b32 v15, 15, v0\n"
      "v_and_b32 v14, 48, v0\n"
      "v_xor_b32 v14, 16, v14\n"
      "v_lshl_or_b32 v14, v15, 6, v14\n"
      "s_waitcnt vmcnt(0)\n"
      "s_mov_b32 s76, 0x8000\n"
      "s_mov_b64 exec, 65535\n"
      ".Lpl_45_%=:\n"
      "v_cmp_ne_u64 vcc, s[58:59], v[248:249]\n"
      "s_cbranch_vccz .Lpd_46_%=\n"
      "s_sub_u32 s76, s76, 1\n"
      "s_cbranch_scc1 .Lpd_46_%=\n"
      "s_sleep 2\n"
      "s_waitcnt vmcnt(0)\n"
      "v_mov_b64 v[248:249], 0\n"
      "buffer_atomic_or_x2 v[248:249], v252, s[16:19], s66 offen sc0 sc1\n"
      "s_waitcnt vmcnt(0)\n"
      "s_branch .Lpl_45_%=\n"
      ".Lpd_46_%=:\n"
      "s_mov_b64 exec, -1\n"
      "s_mov_b64 exec, 15\n"
      "v_mov_b64 v[254:255], 0\n"
      "buffer_atomic_or_x2 v[254:255], v252, s[16:19], s54 offen sc0 sc1\n"
      "s_mov_b64 exec, -1\n"
      "buffer_load_dwordx4 v[244:247], v14, s[16:19], s65 offen sc1\n"
      "buffer_load_dwordx4 v[16:19], v1, s[16:19], s67 offen offset:0 sc1\n"
      "buffer_load_dwordx4 v[20:23], v1, s[16:19], s67 offen offset:1024 sc1\n"
      "buffer_load_dwordx4 v[24:27], v1, s[16:19], s67 offen offset:2048 sc1\n"
      "buffer_load_dwordx4 v[28:31], v1, s[16:19], s67 offen offset:3072 sc1\n"
      "s_add_u32 s67, s67, 4096\n"
      "buffer_load_dwordx4 v[32:35], v1, s[16:19], s67 offen offset:0 sc1\n"
      "buffer_load_dwordx4 v[36:39], v1, s[16:19], s67 offen offset:1024 sc1\n"
      "buffer_load_dwordx4 v[40:43], v1, s[16:19], s67 offen offset:2048 sc1\n"
      "buffer_load_dwordx4 v[44:47], v1, s[16:19], s67 offen offset:3072 sc1\n"
      "s_add_u32 s67, s67, 4096\n"
      "buffer_load_dwordx4 v[48:51], v1, s[16:19], s67 offen offset:0 sc1\n"
      "buffer_load_dwordx4 v[52:55], v1, s[16:19], s67 offen offset:1024 sc1\n"
      "buffer_load_dwordx4 v[56:59], v1, s[16:19], s67 offen offset:2048 sc1\n"
      "buffer_load_dwordx4 v[60:63], v1, s[16:19], s67 offen offset:3072 sc1\n"
      "s_add_u32 s67, s67, 4096\n"
      "buffer_load_dwordx4 v[220:223], v1, s[16:19], s67 offen offset:0 sc1\n"
      "buffer_load_dwordx4 v[224:227], v1, s[16:19], s67 offen offset:1024 sc1\n"
      "buffer_load_dwordx4 v[228:231], v1, s[16:19], s67 offen offset:2048 sc1\n"
      "buffer_load_dwordx4 v[240:243], v1, s[16:19], s67 offen offset:3072 sc1\n"
      "s_and_b32 s62, s99, 1\n"
      "s_lshl_b32 s62, s62, 11\n"
      "s_lshl3_add_u32 s62, s63, s62\n"
      "s_add_u32 s62, s62, 156672\n"
      "v_and_b32 v9, 16, v0\n"
      "v_lshl_or_b32 v9, v9, 6, v1\n"
      "v_xor_b32 v14, 0x400, v9\n"
      "v_add_u32 v9, s62, v9\n"
      "v_add_u32 v14, s62, v14\n"
      "s_waitcnt vmcnt(16)\n"
      "v_add_f32_dpp v244, v244, v244 quad_perm:[1,0,3,2] row_mask:0xf bank_mask:0xf\n"
      "v_add_f32_dpp v245, v245, v245 quad_perm:[1,0,3,2] row_mask:0xf bank_mask:0xf\n"
      "v_add_f32_dpp v246, v246, v246 quad_perm:[1,0,3,2] row_mask:0xf bank_mask:0xf\n"
      "v_add_f32_dpp v247, v247, v247 quad_perm:[1,0,3,2] row_mask:0xf bank_mask:0xf\n"
      "v_add_f32_dpp v244, v244, v244 quad_perm:[2,3,0,1] row_mask:0xf bank_mask:0xf\n"
      "v_add_f32_dpp v245, v245, v245 quad_perm:[2,3,0,1] row_mask:0xf bank_mask:0xf\n"
      "v_add_f32_dpp v246, v246, v246 quad_perm:[2,3,0,1] row_mask:0xf bank_mask:0xf\n"
      "v_add_f32_dpp v247, v247, v247 quad_perm:[2,3,0,1] row_mask:0xf bank_mask:0xf\n"
      "v_add_f32_dpp v244, v244, v244 row_half_mirror row_mask:0xf bank_mask:0xf\n"
      "v_add_f32_dpp v245, v245, v245 row_half_mirror row_mask:0xf bank_mask:0xf\n"
      "v_add_f32_dpp v246, v246, v246 row_half_mirror row_mask:0xf bank_mask:0xf\n"
      "v_add_f32_dpp v247, v247, v247 row_half_mirror row_mask:0xf bank_mask:0xf\n"
      "v_add_f32_dpp v244, v244, v244 row_mirror row_mask:0xf bank_mask:0xf\n"
      "v_add_f32_dpp v245, v245, v245 row_mirror row_mask:0xf bank_mask:0xf\n"
      "v_add_f32_dpp v246, v246, v246 row_mirror row_mask:0xf bank_mask:0xf\n"
      "v_add_f32_dpp v247, v247, v247 row_mirror row_mask:0xf bank_mask:0xf\n"
      "v_cvt_pk_bf16_f32 v248, v244, v245\n"
      "v_cvt_pk_bf16_f32 v249, v246, v247\n"
      "ds_write_b64 v14, v[248:249]\n"
      "s_waitcnt vmcnt(14)\n"
      "v_add_f32 v16, v16, v20\n"
      "v_add_f32 v17, v17, v21\n"
      "v_add_f32 v18, v18, v22\n"
      "v_add_f32 v19, v19, v23\n"
      "s_waitcnt vmcnt(13)\n"
      "v_add_f32 v16, v16, v24\n"
      "v_add_f32 v17, v17, v25\n"
      "v_add_f32 v18, v18, v26\n"
      "v_add_f32 v19, v19, v27\n"
      "s_waitcnt vmcnt(12)\n"
      "v_add_f32 v16, v16, v28\n"
      "v_add_f32 v17, v17, v29\n"
      "v_add_f32 v18, v18, v30\n"
      "v_add_f32 v19, v19, v31\n"
      "s_waitcnt vmcnt(11)\n"
      "v_add_f32 v16, v16, v32\n"
      "v_add_f32 v17, v17, v33\n"
      "v_add_f32 v18, v18, v34\n"
      "v_add_f32 v19, v19, v35\n"
      "s_waitcnt vmcnt(10)\n"
      "v_add_f32 v16, v16, v36\n"
      "v_add_f32 v17, v17, v37\n"
      "v_add_f32 v18, v18, v38\n"
      "v_add_f32 v19, v19, v39\n"
      "s_waitcnt vmcnt(9)\n"
      "v_add_f32 v16, v16, v40\n"
      "v_add_f32 v17, v17, v41\n"
      "v_add_f32 v18, v18, v42\n"
      "v_add_f32 v19, v19, v43\n"
      "s_waitcnt vmcnt(8)\n"
      "v_add_f32 v16, v16, v44\n"
      "v_add_f32 v17, v17, v45\n"
      "v_add_f32 v18, v18, v46\n"
      "v_add_f32 v19, v19, v47\n"
      "s_waitcnt vmcnt(7)\n"
      "v_add_f32 v16, v16, v48\n"
      "v_add_f32 v17, v17, v49\n"
      "v_add_f32 v18, v18, v50\n"
      "v_add_f32 v19, v19, v51\n"
      "s_waitcnt vmcnt(6)\n"
      "v_add_f32 v16, v16, v52\n"
      "v_add_f32 v17, v17, v53\n"
      "v_add_f32 v18, v18, v54\n"
      "v_add_f32 v19, v19, v55\n"
      "s_waitcnt vmcnt(5)\n"
      "v_add_f32 v16, v16, v56\n"
      "v_add_f32 v17, v17, v57\n"
      "v_add_f32 v18, v18, v58\n"
      "v_add_f32 v19, v19, v59\n"
      "s_waitcnt vmcnt(4)\n"
      "v_add_f32 v16, v16, v60\n"
      "v_add_f32 v17, v17, v61\n"
      "v_add_f32 v18, v18, v62\n"
      "v_add_f32 v19, v19, v63\n"
      "s_waitcnt vmcnt(3)\n"
      "v_add_f32 v16, v16, v220\n"
      "v_add_f32 v17, v17, v221\n"
      "v_add_f32 v18, v18, v222\n"
      "v_add_f32 v19, v19, v223\n"
      "s_waitcnt vmcnt(2)\n"
      "v_add_f32 v16, v16, v224\n"
      "v_add_f32 v17, v17, v225\n"
      "v_add_f32 v18, v18, v226\n"
      "v_add_f32 v19, v19, v227\n"
      "s_waitcnt vmcnt(1)\n"
      "v_add_f32 v16, v16, v228\n"
      "v_add_f32 v17, v17, v229\n"
      "v_add_f32 v18, v18, v230\n"
      "v_add_f32 v19, v19, v231\n"
      "s_waitcnt vmcnt(0)\n"
      "v_add_f32 v16, v16, v240\n"
      "v_add_f32 v17, v17, v241\n"
      "v_add_f32 v18, v18, v242\n"
      "v_add_f32 v19, v19, v243\n"
      "v_cvt_pk_bf16_f32 v250, v16, v17\n"
      "v_cvt_pk_bf16_f32 v251, v18, v19\n"
      "ds_write_b64 v9, v[250:251]\n"
      "s_lshl2_add_u32 s70, s99, 160768\n"
      "v_mov_b32 v20, s70\n"
      "v_mov_b32 v21, 4\n"
      "ds_write_b32 v20, v21\n"
      "s_branch .LPA_3_%=\n"
      ".LQB_7_%=:\n"
      "s_setprio 3\n"
      "ds_read_b64_tr_b16 v[64:65], v7 offset:0\n"
      "ds_read_b64_tr_b16 v[66:67], v7 offset:512\n"
      "ds_read_b64_tr_b16 v[68:69], v8 offset:0\n"
      "ds_read_b64_tr_b16 v[70:71], v8 offset:512\n"
      "ds_read_b128 v[72:75], v14 offset:0\n"
      "ds_read_b64_tr_b16 v[104:105], v7 offset:2048\n"
      "ds_read_b64_tr_b16 v[106:107], v7 offset:2560\n"
      "ds_read_b64_tr_b16 v[108:109], v8 offset:2048\n"
      "ds_read_b64_tr_b16 v[110:111], v8 offset:2560\n"
      "ds_read_b128 v[112:115], v14 offset:32\n"
      "s_waitcnt lgkmcnt(5)\n"
      "v_mfma_f32_32x32x16_bf16 v[16:31], v[64:67], v[124:127], v[16:31]\n"
      "ds_read_b64_tr_b16 v[76:77], v7 offset:4096\n"
      "ds_read_b64_tr_b16 v[78:79], v7 offset:4608\n"
      "v_mfma_f32_32x32x16_bf16 v[32:47], v[68:71], v[124:127], v[32:47]\n"
      "ds_read_b64_tr_b16 v[116:117], v8 offset:4096\n"
      "ds_read_b64_tr_b16 v[118:119], v8 offset:4608\n"
      "s_add_u32 s75, s75, 1\n"
      "s_and_b32 s75, s75, 7\n"
      "s_mul_i32 s53, s75, s97\n"
      "s_add_u32 m0, s55, 86016\n"
      "s_add_u32 s52, s53, s43\n"
      "buffer_load_dwordx4 v2, s[24:27], s52 offen lds\n"
      "v_mfma_f32_4x4x4_16b_bf16 v[48:51], v[72:73], v[124:125], v[48:51]\n"
      "v_mfma_f32_4x4x4_16b_bf16 v[52:55], v[74:75], v[126:127], v[52:55]\n"
      "ds_read_b128 v[120:123], v14 offset:64\n"
      "s_waitcnt lgkmcnt(5)\n"
      "v_mfma_f32_32x32x16_bf16 v[16:31], v[104:107], v[128:131], v[16:31]\n"
      "ds_read_b64_tr_b16 v[64:65], v7 offset:6144\n"
      "ds_read_b64_tr_b16 v[66:67], v7 offset:6656\n"
      "v_mfma_f32_32x32x16_bf16 v[32:47], v[108:111], v[128:131], v[32:47]\n"
      "ds_read_b64_tr_b16 v[68:69], v8 offset:6144\n"
      "ds_read_b64_tr_b16 v[70:71], v8 offset:6656\n"
      "s_add_u32 m0, s55, 90112\n"
      "s_add_u32 s71, s52, s77\n"
      "buffer_load_dwordx4 v2, s[24:27], s71 offen lds\n"
      "v_mfma_f32_4x4x4_16b_bf16 v[48:51], v[112:113], v[128:129], v[48:51]\n"
      "v_mfma_f32_4x4x4_16b_bf16 v[52:55], v[114:115], v[130:131], v[52:55]\n"
      "ds_read_b128 v[72:75], v14 offset:96\n"
      "v_add_u32 v14, 128, v14\n"
      "v_and_b32 v14, 0xfffffbff, v14\n"
      "s_waitcnt lgkmcnt(5)\n"
      "v_mfma_f32_32x32x16_bf16 v[16:31], v[76:79], v[132:135], v[16:31]\n"
      "ds_read_b64_tr_b16 v[104:105], v7 offset:12288\n"
      "ds_read_b64_tr_b16 v[106:107], v7 offset:12800\n"
      "v_mfma_f32_32x32x16_bf16 v[32:47], v[116:119], v[132:135], v[32:47]\n"
      "ds_read_b64_tr_b16 v[108:109], v8 offset:12288\n"
      "ds_read_b64_tr_b16 v[110:111], v8 offset:12800\n"
      "s_add_u32 m0, s96, 98304\n"
      "s_nop 0\n"
      "buffer_load_dwordx4 v4, s[8:11], s28 offen offset:0 lds\n"
      "v_mfma_f32_4x4x4_16b_bf16 v[48:51], v[120:121], v[132:133], v[48:51]\n"
      "v_mfma_f32_4x4x4_16b_bf16 v[52:55], v[122:123], v[134:135], v[52:55]\n"
      "ds_read_b128 v[112:115], v14 offset:0\n"
      "s_waitcnt lgkmcnt(5)\n"
      "v_mfma_f32_32x32x16_bf16 v[16:31], v[64:67], v[136:139], v[16:31]\n"
      "ds_read_b64_tr_b16 v[76:77], v7 offset:14336\n"
      "ds_read_b64_tr_b16 v[78:79], v7 offset:14848\n"
      "v_mfma_f32_32x32x16_bf16 v[32:47], v[68:71], v[136:139], v[32:47]\n"
      "ds_read_b64_tr_b16 v[116:117], v8 offset:14336\n"
      "ds_read_b64_tr_b16 v[118:119], v8 offset:14848\n"
      "buffer_load_dwordx4 v96, s[8:11], s28 offen offset:1024 lds\n"
      "v_mfma_f32_4x4x4_16b_bf16 v[48:51], v[72:73], v[136:137], v[48:51]\n"
      "v_mfma_f32_4x4x4_16b_bf16 v[52:55], v[74:75], v[138:139], v[52:55]\n"
      "ds_read_b128 v[120:123], v14 offset:32\n"
      "s_waitcnt lgkmcnt(5)\n"
      "v_mfma_f32_32x32x16_bf16 v[16:31], v[104:107], v[140:143], v[16:31]\n"
      "ds_read_b64_tr_b16 v[64:65], v7 offset:16384\n"
      "ds_read_b64_tr_b16 v[66:67], v7 offset:16896\n"
      "v_mfma_f32_32x32x16_bf16 v[32:47], v[108:111], v[140:143], v[32:47]\n"
      "ds_read_b64_tr_b16 v[68:69], v8 offset:16384\n"
      "ds_read_b64_tr_b16 v[70:71], v8 offset:16896\n"
      "buffer_load_dwordx4 v97, s[8:11], s28 offen offset:2048 lds\n"
      "v_mfma_f32_4x4x4_16b_bf16 v[48:51], v[112:113], v[140:141], v[48:51]\n"
      "v_mfma_f32_4x4x4_16b_bf16 v[52:55], v[114:115], v[142:143], v[52:55]\n"
      "ds_read_b128 v[72:75], v14 offset:64\n"
      "s_waitcnt lgkmcnt(5)\n"
      "v_mfma_f32_32x32x16_bf16 v[16:31], v[76:79], v[144:147], v[16:31]\n"
      "ds_read_b64_tr_b16 v[104:105], v7 offset:18432\n"
      "ds_read_b64_tr_b16 v[106:107], v7 offset:18944\n"
      "v_mfma_f32_32x32x16_bf16 v[32:47], v[116:119], v[144:147], v[32:47]\n"
      "ds_read_b64_tr_b16 v[108:109], v8 offset:18432\n"
      "ds_read_b64_tr_b16 v[110:111], v8 offset:18944\n"
      "buffer_load_dwordx4 v98, s[8:11], s28 offen offset:3072 lds\n"
      "v_mfma_f32_4x4x4_16b_bf16 v[48:51], v[120:121], v[144:145], v[48:51]\n"
      "v_mfma_f32_4x4x4_16b_bf16 v[52:55], v[122:123], v[146:147], v[52:55]\n"
      "ds_read_b128 v[112:115], v14 offset:96\n"
      "v_add_u32 v14, 128, v14\n"
      "v_and_b32 v14, 0xfffffbff, v14\n"
      "s_waitcnt lgkmcnt(5)\n"
      "v_mfma_f32_32x32x16_bf16 v[16:31], v[64:67], v[148:151], v[16:31]\n"
      "ds_read_b64_tr_b16 v[76:77], v7 offset:24576\n"
      "ds_read_b64_tr_b16 v[78:79], v7 offset:25088\n"
      "v_mfma_f32_32x32x16_bf16 v[32:47], v[68:71], v[148:151], v[32:47]\n"
      "ds_read_b64_tr_b16 v[116:117], v8 offset:24576\n"
      "ds_read_b64_tr_b16 v[118:119], v8 offset:25088\n"
      "s_add_u32 m0, s96, 102400\n"
      "s_nop 0\n"
      "buffer_load_dwordx4 v4, s[8:11], s29 offen offset:0 lds\n"
      "v_mfma_f32_4x4x4_16b_bf16 v[48:51], v[72:73], v[148:149], v[48:51]\n"
      "v_mfma_f32_4x4x4_16b_bf16 v[52:55], v[74:75], v[150:151], v[52:55]\n"
      "ds_read_b128 v[120:123], v14 offset:0\n"
      "s_waitcnt lgkmcnt(5)\n"
      "v_mfma_f32_32x32x16_bf16 v[16:31], v[104:107], v[152:155], v[16:31]\n"
      "ds_read_b64_tr_b16 v[64:65], v7 offset:26624\n"
      "ds_read_b64_tr_b16 v[66:67], v7 offset:27136\n"
      "v_mfma_f32_32x32x16_bf16 v[32:47], v[108:111], v[152:155], v[32:47]\n"
      "ds_read_b64_tr_b16 v[68:69], v8 offset:26624\n"
      "ds_read_b64_tr_b16 v[70:71], v8 offset:27136\n"
      "buffer_load_dwordx4 v96, s[8:11], s29 offen offset:1024 lds\n"
      "v_mfma_f32_4x4x4_16b_bf16 v[48:51], v[112:113], v[152:153], v[48:51]\n"
      "v_mfma_f32_4x4x4_16b_bf16 v[52:55], v[114:115], v[154:155], v[52:55]\n"
      "ds_read_b128 v[72:75], v14 offset:32\n"
      "s_waitcnt lgkmcnt(5)\n"
      "v_mfma_f32_32x32x16_bf16 v[16:31], v[76:79], v[156:159], v[16:31]\n"
      "s_waitcnt vmcnt(6)\n"
      "ds_write_b32 v99, v101\n"
      "ds_read_b64_tr_b16 v[104:105], v7 offset:28672\n"
      "ds_read_b64_tr_b16 v[106:107], v7 offset:29184\n"
      "v_mfma_f32_32x32x16_bf16 v[32:47], v[116:119], v[156:159], v[32:47]\n"
      "ds_read_b64_tr_b16 v[108:109], v8 offset:28672\n"
      "ds_read_b64_tr_b16 v[110:111], v8 offset:29184\n"
      "buffer_load_dwordx4 v97, s[8:11], s29 offen offset:2048 lds\n"
      "v_mfma_f32_4x4x4_16b_bf16 v[48:51], v[120:121], v[156:157], v[48:51]\n"
      "v_mfma_f32_4x4x4_16b_bf16 v[52:55], v[122:123], v[158:159], v[52:55]\n"
      "ds_read_b128 v[112:115], v14 offset:64\n"
      "s_waitcnt lgkmcnt(5)\n"
      "v_mfma_f32_32x32x16_bf16 v[16:31], v[64:67], v[160:163], v[16:31]\n"
      "ds_read_b64_tr_b16 v[76:77], v7 offset:30720\n"
      "ds_read_b64_tr_b16 v[78:79], v7 offset:31232\n"
      "v_mfma_f32_32x32x16_bf16 v[32:47], v[68:71], v[160:163], v[32:47]\n"
      "ds_read_b64_tr_b16 v[116:117], v8 offset:30720\n"
      "ds_read_b64_tr_b16 v[118:119], v8 offset:31232\n"
      "buffer_load_dwordx4 v98, s[8:11], s29 offen offset:3072 lds\n"
      "v_mfma_f32_4x4x4_16b_bf16 v[48:51], v[72:73], v[160:161], v[48:51]\n"
      "v_mfma_f32_4x4x4_16b_bf16 v[52:55], v[74:75], v[162:163], v[52:55]\n"
      "ds_read_b128 v[120:123], v14 offset:96\n"
      "v_add_u32 v14, 128, v14\n"
      "v_and_b32 v14, 0xfffffbff, v14\n"
      "s_waitcnt lgkmcnt(5)\n"
      "v_mfma_f32_32x32x16_bf16 v[16:31], v[104:107], v[164:167], v[16:31]\n"
      "ds_read_b64_tr_b16 v[64:65], v7 offset:36864\n"
      "ds_read_b64_tr_b16 v[66:67], v7 offset:37376\n"
      "v_mfma_f32_32x32x16_bf16 v[32:47], v[108:111], v[164:167], v[32:47]\n"
      "ds_read_b64_tr_b16 v[68:69], v8 offset:36864\n"
      "ds_read_b64_tr_b16 v[70:71], v8 offset:37376\n"
      "s_add_u32 m0, s96, 106496\n"
      "s_nop 0\n"
      "buffer_load_dwordx4 v4, s[8:11], s30 offen offset:0 lds\n"
      "v_mfma_f32_4x4x4_16b_bf16 v[48:51], v[112:113], v[164:165], v[48:51]\n"
      "v_mfma_f32_4x4x4_16b_bf16 v[52:55], v[114:115], v[166:167], v[52:55]\n"
      "ds_read_b128 v[72:75], v14 offset:0\n"
      "s_waitcnt lgkmcnt(5)\n"
      "v_mfma_f32_32x32x16_bf16 v[16:31], v[76:79], v[168:171], v[16:31]\n"
      "ds_read_b64_tr_b16 v[104:105], v7 offset:38912\n"
      "ds_read_b64_tr_b16 v[106:107], v7 offset:39424\n"
      "v_mfma_f32_32x32x16_bf16 v[32:47], v[116:119], v[168:171], v[32:47]\n"
      "ds_read_b64_tr_b16 v[108:109], v8 offset:38912\n"
      "ds_read_b64_tr_b16 v[110:111], v8 offset:39424\n"
      "buffer_load_dwordx4 v96, s[8:11], s30 offen offset:1024 lds\n"
      "v_mfma_f32_4x4x4_16b_bf16 v[48:51], v[120:121], v[168:169], v[48:51]\n"
      "v_mfma_f32_4x4x4_16b_bf16 v[52:55], v[122:123], v[170:171], v[52:55]\n"
      "ds_read_b128 v[112:115], v14 offset:32\n"
      "s_waitcnt lgkmcnt(5)\n"
      "v_mfma_f32_32x32x16_bf16 v[16:31], v[64:67], v[172:175], v[16:31]\n"
      "ds_read_b128 v[88:91], v100\n"
      "ds_read_b64_tr_b16 v[76:77], v7 offset:40960\n"
      "ds_read_b64_tr_b16 v[78:79], v7 offset:41472\n"
      "v_mfma_f32_32x32x16_bf16 v[32:47], v[68:71], v[172:175], v[32:47]\n"
      "ds_read_b64_tr_b16 v[116:117], v8 offset:40960\n"
      "ds_read_b64_tr_b16 v[118:119], v8 offset:41472\n"
      "buffer_load_dwordx4 v97, s[8:11], s30 offen offset:2048 lds\n"
      "v_mfma_f32_4x4x4_16b_bf16 v[48:51], v[72:73], v[172:173], v[48:51]\n"
      "v_mfma_f32_4x4x4_16b_bf16 v[52:55], v[74:75], v[174:175], v[52:55]\n"
      "ds_read_b128 v[120:123], v14 offset:64\n"
      "s_waitcnt lgkmcnt(5)\n"
      "v_mfma_f32_32x32x16_bf16 v[16:31], v[104:107], v[176:179], v[16:31]\n"
      "v_min3_u32 v88, v88, v89, v90\n"
      "v_min_u32 v88, v88, v91\n"
      "v_cmp_le_u32 vcc, 1, v88\n"
      "s_cbranch_vccz .Llz_47_%=\n"
      ".Llzr_48_%=:\n"
      "ds_read_b64_tr_b16 v[64:65], v7 offset:43008\n"
      "ds_read_b64_tr_b16 v[66:67], v7 offset:43520\n"
      "v_mfma_f32_32x32x16_bf16 v[32:47], v[108:111], v[176:179], v[32:47]\n"
      "ds_read_b64_tr_b16 v[68:69], v8 offset:43008\n"
      "ds_read_b64_tr_b16 v[70:71], v8 offset:43520\n"
      "buffer_load_dwordx4 v98, s[8:11], s30 offen offset:3072 lds\n"
      "v_mfma_f32_4x4x4_16b_bf16 v[48:51], v[112:113], v[176:177], v[48:51]\n"
      "v_mfma_f32_4x4x4_16b_bf16 v[52:55], v[114:115], v[178:179], v[52:55]\n"
      "ds_read_b128 v[72:75], v14 offset:96\n"
      "v_add_u32 v14, 128, v14\n"
      "v_and_b32 v14, 0xfffffbff, v14\n"
      "s_waitcnt lgkmcnt(5)\n"
      "v_mfma_f32_32x32x16_bf16 v[16:31], v[76:79], v[180:183], v[16:31]\n"
      "v_add_u32 v7, 49152, v7\n"
      "v_add_u32 v8, 49152, v8\n"
      "ds_read_b64_tr_b16 v[104:105], v7 offset:0\n"
      "ds_read_b64_tr_b16 v[106:107], v7 offset:512\n"
      "v_mfma_f32_32x32x16_bf16 v[32:47], v[116:119], v[180:183], v[32:47]\n"
      "ds_read_b64_tr_b16 v[108:109], v8 offset:0\n"
      "ds_read_b64_tr_b16 v[110:111], v8 offset:512\n"
      "v_mfma_f32_4x4x4_16b_bf16 v[48:51], v[120:121], v[180:181], v[48:51]\n"
      "v_mfma_f32_4x4x4_16b_bf16 v[52:55], v[122:123], v[182:183], v[52:55]\n"
      "ds_read_b128 v[112:115], v14 offset:0\n"
      "s_waitcnt lgkmcnt(5)\n"
      "v_mfma_f32_32x32x16_bf16 v[16:31], v[64:67], v[184:187], v[16:31]\n"
      "ds_write_b32 v99, v102\n"
      "ds_read_b64_tr_b16 v[76:77], v7 offset:2048\n"
      "ds_read_b64_tr_b16 v[78:79], v7 offset:2560\n"
      "v_mfma_f32_32x32x16_bf16 v[32:47], v[68:71], v[184:187], v[32:47]\n"
      "ds_read_b64_tr_b16 v[116:117], v8 offset:2048\n"
      "ds_read_b64_tr_b16 v[118:119], v8 offset:2560\n"
      "v_mfma_f32_4x4x4_16b_bf16 v[48:51], v[72:73], v[184:185], v[48:51]\n"
      "v_mfma_f32_4x4x4_16b_bf16 v[52:55], v[74:75], v[186:187], v[52:55]\n"
      "ds_read_b128 v[120:123], v14 offset:32\n"
      "s_waitcnt lgkmcnt(5)\n"
      "v_mfma_f32_32x32x16_bf16 v[16:31], v[104:107], v[188:191], v[16:31]\n"
      "ds_read_b64_tr_b16 v[64:65], v7 offset:4096\n"
      "ds_read_b64_tr_b16 v[66:67], v7 offset:4608\n"
      "v_mfma_f32_32x32x16_bf16 v[32:47], v[108:111], v[188:191], v[32:47]\n"
      "ds_read_b64_tr_b16 v[68:69], v8 offset:4096\n"
      "ds_read_b64_tr_b16 v[70:71], v8 offset:4608\n"
      "v_mfma_f32_4x4x4_16b_bf16 v[48:51], v[112:113], v[188:189], v[48:51]\n"
      "v_mfma_f32_4x4x4_16b_bf16 v[52:55], v[114:115], v[190:191], v[52:55]\n"
      "ds_read_b128 v[72:75], v14 offset:64\n"
      "s_waitcnt lgkmcnt(5)\n"
      "v_mfma_f32_32x32x16_bf16 v[16:31], v[76:79], v[192:195], v[16:31]\n"
      "ds_read_b128 v[88:91], v100\n"
      "ds_read_b64_tr_b16 v[104:105], v7 offset:6144\n"
      "ds_read_b64_tr_b16 v[106:107], v7 offset:6656\n"
      "v_mfma_f32_32x32x16_bf16 v[32:47], v[116:119], v[192:195], v[32:47]\n"
      "ds_read_b64_tr_b16 v[108:109], v8 offset:6144\n"
      "ds_read_b64_tr_b16 v[110:111], v8 offset:6656\n"
      "v_mfma_f32_4x4x4_16b_bf16 v[48:51], v[120:121], v[192:193], v[48:51]\n"
      "v_mfma_f32_4x4x4_16b_bf16 v[52:55], v[122:123], v[194:195], v[52:55]\n"
      "ds_read_b128 v[112:115], v14 offset:96\n"
      "v_add_u32 v14, 128, v14\n"
      "v_and_b32 v14, 0xfffffbff, v14\n"
      "s_waitcnt lgkmcnt(5)\n"
      "v_mfma_f32_32x32x16_bf16 v[16:31], v[64:67], v[196:199], v[16:31]\n"
      "v_min3_u32 v88, v88, v89, v90\n"
      "v_min_u32 v88, v88, v91\n"
      "v_cmp_le_u32 vcc, 2, v88\n"
      "s_cbranch_vccz .Llz_51_%=\n"
      ".Llzr_52_%=:\n"
      "ds_read_b64_tr_b16 v[76:77], v7 offset:12288\n"
      "ds_read_b64_tr_b16 v[78:79], v7 offset:12800\n"
      "v_mfma_f32_32x32x16_bf16 v[32:47], v[68:71], v[196:199], v[32:47]\n"
      "ds_read_b64_tr_b16 v[116:117], v8 offset:12288\n"
      "ds_read_b64_tr_b16 v[118:119], v8 offset:12800\n"
      "s_add_u32 m0, s96, 0\n"
      "s_nop 0\n"
      "buffer_load_dwordx4 v4, s[8:11], s31 offen offset:0 lds\n"
      "v_mfma_f32_4x4x4_16b_bf16 v[48:51], v[72:73], v[196:197], v[48:51]\n"
      "v_mfma_f32_4x4x4_16b_bf16 v[52:55], v[74:75], v[198:199], v[52:55]\n"
      "ds_read_b128 v[120:123], v14 offset:0\n"
      "s_waitcnt lgkmcnt(5)\n"
      "v_mfma_f32_32x32x16_bf16 v[16:31], v[104:107], v[200:203], v[16:31]\n"
      "ds_read_b64_tr_b16 v[64:65], v7 offset:14336\n"
      "ds_read_b64_tr_b16 v[66:67], v7 offset:14848\n"
      "v_mfma_f32_32x32x16_bf16 v[32:47], v[108:111], v[200:203], v[32:47]\n"
      "ds_read_b64_tr_b16 v[68:69], v8 offset:14336\n"
      "ds_read_b64_tr_b16 v[70:71], v8 offset:14848\n"
      "buffer_load_dwordx4 v96, s[8:11], s31 offen offset:1024 lds\n"
      "v_mfma_f32_4x4x4_16b_bf16 v[48:51], v[112:113], v[200:201], v[48:51]\n"
      "v_mfma_f32_4x4x4_16b_bf16 v[52:55], v[114:115], v[202:203], v[52:55]\n"
      "ds_read_b128 v[72:75], v14 offset:32\n"
      "buffer_load_dwordx4 v97, s[8:11], s31 offen offset:2048 lds\n"
      "s_waitcnt lgkmcnt(5)\n"
      "v_mfma_f32_32x32x16_bf16 v[16:31], v[76:79], v[204:207], v[16:31]\n"
      "ds_read_b64_tr_b16 v[104:105], v7 offset:16384\n"
      "ds_read_b64_tr_b16 v[106:107], v7 offset:16896\n"
      "v_mfma_f32_32x32x16_bf16 v[32:47], v[116:119], v[204:207], v[32:47]\n"
      "ds_read_b64_tr_b16 v[108:109], v8 offset:16384\n"
      "ds_read_b64_tr_b16 v[110:111], v8 offset:16896\n"
      "buffer_load_dwordx4 v98, s[8:11], s31 offen offset:3072 lds\n"
      "v_mfma_f32_4x4x4_16b_bf16 v[48:51], v[120:121], v[204:205], v[48:51]\n"
      "v_mfma_f32_4x4x4_16b_bf16 v[52:55], v[122:123], v[206:207], v[52:55]\n"
      "ds_read_b128 v[112:115], v14 offset:64\n"
      "v_mov_b32 v12, s58\n"
      "s_waitcnt lgkmcnt(5)\n"
      "v_mfma_f32_32x32x16_bf16 v[16:31], v[64:67], v[208:211], v[16:31]\n"
      "ds_read_b64_tr_b16 v[76:77], v7 offset:18432\n"
      "ds_read_b64_tr_b16 v[78:79], v7 offset:18944\n"
      "v_mfma_f32_32x32x16_bf16 v[32:47], v[68:71], v[208:211], v[32:47]\n"
      "ds_read_b64_tr_b16 v[116:117], v8 offset:18432\n"
      "ds_read_b64_tr_b16 v[118:119], v8 offset:18944\n"
      "s_add_u32 m0, s96, 4096\n"
      "s_nop 0\n"
      "buffer_load_dwordx4 v4, s[8:11], s0 offen offset:0 lds\n"
      "v_mfma_f32_4x4x4_16b_bf16 v[48:51], v[72:73], v[208:209], v[48:51]\n"
      "v_mfma_f32_4x4x4_16b_bf16 v[52:55], v[74:75], v[210:211], v[52:55]\n"
      "ds_read_b128 v[120:123], v14 offset:96\n"
      "buffer_load_dwordx4 v96, s[8:11], s0 offen offset:1024 lds\n"
      "v_add_u32 v14, 128, v14\n"
      "v_and_b32 v14, 0xfffffbff, v14\n"
      "s_waitcnt lgkmcnt(5)\n"
      "v_mfma_f32_32x32x16_bf16 v[16:31], v[104:107], v[212:215], v[16:31]\n"
      "ds_read_b64_tr_b16 v[64:65], v7 offset:24576\n"
      "ds_read_b64_tr_b16 v[66:67], v7 offset:25088\n"
      "v_mfma_f32_32x32x16_bf16 v[32:47], v[108:111], v[212:215], v[32:47]\n"
      "ds_read_b64_tr_b16 v[68:69], v8 offset:24576\n"
      "ds_read_b64_tr_b16 v[70:71], v8 offset:25088\n"
      "buffer_load_dwordx4 v97, s[8:11], s0 offen offset:2048 lds\n"
      "v_mfma_f32_4x4x4_16b_bf16 v[48:51], v[112:113], v[212:213], v[48:51]\n"
      "v_mfma_f32_4x4x4_16b_bf16 v[52:55], v[114:115], v[214:215], v[52:55]\n"
      "ds_read_b128 v[72:75], v14 offset:0\n"
      "v_mov_b32 v13, s59\n"
      "s_waitcnt lgkmcnt(5)\n"
      "v_mfma_f32_32x32x16_bf16 v[16:31], v[76:79], v[216:219], v[16:31]\n"
      "ds_read_b64_tr_b16 v[104:105], v7 offset:26624\n"
      "ds_read_b64_tr_b16 v[106:107], v7 offset:27136\n"
      "v_mfma_f32_32x32x16_bf16 v[32:47], v[116:119], v[216:219], v[32:47]\n"
      "ds_read_b64_tr_b16 v[108:109], v8 offset:26624\n"
      "ds_read_b64_tr_b16 v[110:111], v8 offset:27136\n"
      "buffer_load_dwordx4 v98, s[8:11], s0 offen offset:3072 lds\n"
      "v_mfma_f32_4x4x4_16b_bf16 v[48:51], v[120:121], v[216:217], v[48:51]\n"
      "v_mfma_f32_4x4x4_16b_bf16 v[52:55], v[122:123], v[218:219], v[52:55]\n"
      "ds_read_b128 v[112:115], v14 offset:32\n"
      "s_add_u32 m0, s96, 8192\n"
      "s_nop 0\n"
      "buffer_load_dwordx4 v4, s[8:11], s1 offen offset:0 lds\n"
      "s_waitcnt lgkmcnt(5)\n"
      "v_mfma_f32_32x32x16_bf16 v[16:31], v[64:67], v[220:223], v[16:31]\n"
      "ds_read_b64_tr_b16 v[76:77], v7 offset:28672\n"
      "ds_read_b64_tr_b16 v[78:79], v7 offset:29184\n"
      "v_mfma_f32_32x32x16_bf16 v[32:47], v[68:71], v[220:223], v[32:47]\n"
      "ds_read_b64_tr_b16 v[116:117], v8 offset:28672\n"
      "ds_read_b64_tr_b16 v[118:119], v8 offset:29184\n"
      "buffer_load_dwordx4 v96, s[8:11], s1 offen offset:1024 lds\n"
      "v_mfma_f32_4x4x4_16b_bf16 v[48:51], v[72:73], v[220:221], v[48:51]\n"
      "v_mfma_f32_4x4x4_16b_bf16 v[52:55], v[74:75], v[222:223], v[52:55]\n"
      "ds_read_b128 v[120:123], v14 offset:64\n"
      "s_and_b32 s69, s99, 2\n"
      "s_waitcnt lgkmcnt(5)\n"
      "v_mfma_f32_32x32x16_bf16 v[16:31], v[104:107], v[224:227], v[16:31]\n"
      "s_waitcnt vmcnt(6)\n"
      "ds_write_b32 v99, v101 offset:48\n"
      "ds_read_b64_tr_b16 v[64:65], v7 offset:30720\n"
      "ds_read_b64_tr_b16 v[66:67], v7 offset:31232\n"
      "v_mfma_f32_32x32x16_bf16 v[32:47], v[108:111], v[224:227], v[32:47]\n"
      "ds_read_b64_tr_b16 v[68:69], v8 offset:30720\n"
      "ds_read_b64_tr_b16 v[70:71], v8 offset:31232\n"
      "buffer_load_dwordx4 v97, s[8:11], s1 offen offset:2048 lds\n"
      "v_mfma_f32_4x4x4_16b_bf16 v[48:51], v[112:113], v[224:225], v[48:51]\n"
      "v_mfma_f32_4x4x4_16b_bf16 v[52:55], v[114:115], v[226:227], v[52:55]\n"
      "ds_read_b128 v[72:75], v14 offset:96\n"
      "buffer_load_dwordx4 v98, s[8:11], s1 offen offset:3072 lds\n"
      "v_add_u32 v14, 128, v14\n"
      "v_and_b32 v14, 0xfffffbff, v14\n"
      "s_waitcnt lgkmcnt(5)\n"
      "v_mfma_f32_32x32x16_bf16 v[16:31], v[76:79], v[228:231], v[16:31]\n"
      "ds_read_b64_tr_b16 v[104:105], v7 offset:36864\n"
      "ds_read_b64_tr_b16 v[106:107], v7 offset:37376\n"
      "v_mfma_f32_32x32x16_bf16 v[32:47], v[116:119], v[228:231], v[32:47]\n"
      "ds_read_b64_tr_b16 v[108:109], v8 offset:36864\n"
      "ds_read_b64_tr_b16 v[110:111], v8 offset:37376\n"
      "v_mfma_f32_4x4x4_16b_bf16 v[48:51], v[120:121], v[228:229], v[48:51]\n"
      "v_mfma_f32_4x4x4_16b_bf16 v[52:55], v[122:123], v[230:231], v[52:55]\n"
      "ds_read_b128 v[112:115], v14 offset:0\n"
      "s_mul_i32 s69, s69, 12288\n"
      "s_waitcnt lgkmcnt(5)\n"
      "v_mfma_f32_32x32x16_bf16 v[16:31], v[64:67], v[232:235], v[16:31]\n"
      "ds_read_b64_tr_b16 v[76:77], v7 offset:38912\n"
      "ds_read_b64_tr_b16 v[78:79], v7 offset:39424\n"
      "v_mfma_f32_32x32x16_bf16 v[32:47], v[68:71], v[232:235], v[32:47]\n"
      "ds_read_b64_tr_b16 v[116:117], v8 offset:38912\n"
      "ds_read_b64_tr_b16 v[118:119], v8 offset:39424\n"
      "v_mfma_f32_4x4x4_16b_bf16 v[48:51], v[72:73], v[232:233], v[48:51]\n"
      "v_mfma_f32_4x4x4_16b_bf16 v[52:55], v[74:75], v[234:235], v[52:55]\n"
      "ds_read_b128 v[120:123], v14 offset:32\n"
      "v_lshlrev_b32 v1, 4, v0\n"
      "s_waitcnt lgkmcnt(5)\n"
      "v_mfma_f32_32x32x16_bf16 v[16:31], v[104:107], v[236:239], v[16:31]\n"
      "ds_read_b64_tr_b16 v[64:65], v7 offset:40960\n"
      "ds_read_b64_tr_b16 v[66:67], v7 offset:41472\n"
      "v_mfma_f32_32x32x16_bf16 v[32:47], v[108:111], v[236:239], v[32:47]\n"
      "ds_read_b64_tr_b16 v[68:69], v8 offset:40960\n"
      "ds_read_b64_tr_b16 v[70:71], v8 offset:41472\n"
      "v_mfma_f32_4x4x4_16b_bf16 v[48:51], v[112:113], v[236:237], v[48:51]\n"
      "v_mfma_f32_4x4x4_16b_bf16 v[52:55], v[114:115], v[238:239], v[52:55]\n"
      "ds_read_b128 v[72:75], v14 offset:64\n"
      "v_add_u32 v2, s69, v253\n"
      "s_waitcnt lgkmcnt(5)\n"
      "v_mfma_f32_32x32x16_bf16 v[16:31], v[76:79], v[240:243], v[16:31]\n"
      "ds_read_b64_tr_b16 v[104:105], v7 offset:43008\n"
      "ds_read_b64_tr_b16 v[106:107], v7 offset:43520\n"
      "v_mfma_f32_32x32x16_bf16 v[32:47], v[116:119], v[240:243], v[32:47]\n"
      "ds_read_b64_tr_b16 v[108:109], v8 offset:43008\n"
      "ds_read_b64_tr_b16 v[110:111], v8 offset:43520\n"
      "v_mfma_f32_4x4x4_16b_bf16 v[48:51], v[120:121], v[240:241], v[48:51]\n"
      "v_mfma_f32_4x4x4_16b_bf16 v[52:55], v[122:123], v[242:243], v[52:55]\n"
      "ds_read_b128 v[112:115], v14 offset:96\n"
      "v_xor_b32 v3, 64, v2\n"
      "v_add_u32 v14, 128, v14\n"
      "v_and_b32 v14, 0xfffffbff, v14\n"
      "s_waitcnt lgkmcnt(5)\n"
      "v_mfma_f32_32x32x16_bf16 v[16:31], v[64:67], v[244:247], v[16:31]\n"
      "v_mfma_f32_32x32x16_bf16 v[32:47], v[68:71], v[244:247], v[32:47]\n"
      "v_mfma_f32_4x4x4_16b_bf16 v[48:51], v[72:73], v[244:245], v[48:51]\n"
      "v_mfma_f32_4x4x4_16b_bf16 v[52:55], v[74:75], v[246:247], v[52:55]\n"
      "v_add_u32 v5, 49152, v2\n"
      "s_waitcnt lgkmcnt(0)\n"
      "v_mfma_f32_32x32x16_bf16 v[16:31], v[104:107], v[248:251], v[16:31]\n"
      "ds_write_b32 v99, v103\n"
      "v_mfma_f32_32x32x16_bf16 v[32:47], v[108:111], v[248:251], v[32:47]\n"
      "v_mfma_f32_4x4x4_16b_bf16 v[48:51], v[112:113], v[248:249], v[48:51]\n"
      "v_mfma_f32_4x4x4_16b_bf16 v[52:55], v[114:115], v[250:251], v[52:55]\n"
      "v_add_u32 v6, 49152, v3\n"
      "s_mov_b32 s68, 0x358637bd\n"
      "s_mov_b32 s69, 0x358637bd\n"
      "v_lshlrev_b32 v7, 10, v0\n"
      "s_setprio 3\n"
      "s_nop 6\n"
      "v_add_f32 v8, v48, v52\n"
      "v_mul_f32 v152, s60, v16\n"
      "v_mul_f32 v153, s60, v17\n"
      "v_mul_f32 v154, s60, v18\n"
      "v_mul_f32 v155, s60, v19\n"
      "v_mul_f32 v156, s60, v20\n"
      "v_mul_f32 v157, s60, v21\n"
      "v_mul_f32 v158, s60, v22\n"
      "v_mul_f32 v159, s60, v23\n"
      "v_mul_f32 v160, s60, v24\n"
      "v_mul_f32 v161, s60, v25\n"
      "v_mul_f32 v162, s60, v26\n"
      "v_mul_f32 v163, s60, v27\n"
      "v_mul_f32 v164, s60, v28\n"
      "v_mul_f32 v165, s60, v29\n"
      "v_mul_f32 v166, s60, v30\n"
      "v_mul_f32 v167, s60, v31\n"
      "v_exp_f32_e64 v152, v152 clamp\n"
      "v_exp_f32_e64 v153, v153 clamp\n"
      "v_exp_f32_e64 v154, v154 clamp\n"
      "v_exp_f32_e64 v155, v155 clamp\n"
      "v_exp_f32_e64 v156, v156 clamp\n"
      "v_exp_f32_e64 v157, v157 clamp\n"
      "v_exp_f32_e64 v158, v158 clamp\n"
      "v_exp_f32_e64 v159, v159 clamp\n"
      "v_exp_f32_e64 v160, v160 clamp\n"
      "v_exp_f32_e64 v161, v161 clamp\n"
      "v_exp_f32_e64 v162, v162 clamp\n"
      "v_exp_f32_e64 v163, v163 clamp\n"
      "v_exp_f32_e64 v164, v164 clamp\n"
      "v_exp_f32_e64 v165, v165 clamp\n"
      "v_exp_f32_e64 v166, v166 clamp\n"
      "v_exp_f32_e64 v167, v167 clamp\n"
      "v_max_f32 v16, 0, v16\n"
      "v_max_f32 v17, 0, v17\n"
      "v_max_f32 v18, 0, v18\n"
      "v_max_f32 v19, 0, v19\n"
      "v_max_f32 v20, 0, v20\n"
      "v_max_f32 v21, 0, v21\n"
      "v_max_f32 v22, 0, v22\n"
      "v_max_f32 v23, 0, v23\n"
      "v_max_f32 v24, 0, v24\n"
      "v_max_f32 v25, 0, v25\n"
      "v_max_f32 v26, 0, v26\n"
      "v_max_f32 v27, 0, v27\n"
      "v_max_f32 v28, 0, v28\n"
      "v_max_f32 v29, 0, v29\n"
      "v_max_f32 v30, 0, v30\n"
      "v_max_f32 v31, 0, v31\n"
      "v_add_f32 v152, v152, v16\n"
      "v_add_f32 v153, v153, v17\n"
      "v_add_f32 v154, v154, v18\n"
      "v_add_f32 v155, v155, v19\n"
      "v_add_f32 v156, v156, v20\n"
      "v_add_f32 v157, v157, v21\n"
      "v_add_f32 v158, v158, v22\n"
      "v_add_f32 v159, v159, v23\n"
      "v_add_f32 v160, v160, v24\n"
      "v_add_f32 v161, v161, v25\n"
      "v_add_f32 v162, v162, v26\n"
      "v_add_f32 v163, v163, v27\n"
      "v_add_f32 v164, v164, v28\n"
      "v_add_f32 v165, v165, v29\n"
      "v_add_f32 v166, v166, v30\n"
      "v_add_f32 v167, v167, v31\n"
      "v_add_f32 v152, s68, v152\n"
      "v_add_f32 v153, s68, v153\n"
      "v_add_f32 v154, s68, v154\n"
      "v_add_f32 v155, s68, v155\n"
      "v_add_f32 v156, s68, v156\n"
      "v_add_f32 v157, s68, v157\n"
      "v_add_f32 v158, s68, v158\n"
      "v_add_f32 v159, s68, v159\n"
      "v_add_f32 v160, s68, v160\n"
      "v_add_f32 v161, s68, v161\n"
      "v_add_f32 v162, s68, v162\n"
      "v_add_f32 v163, s68, v163\n"
      "v_add_f32 v164, s68, v164\n"
      "v_add_f32 v165, s68, v165\n"
      "v_add_f32 v166, s68, v166\n"
      "v_add_f32 v167, s68, v167\n"
      "v_cvt_pk_bf16_f32 v216, v152, v153\n"
      "v_cvt_pk_bf16_f32 v217, v154, v155\n"
      "v_cvt_pk_bf16_f32 v218, v156, v157\n"
      "v_cvt_pk_bf16_f32 v219, v158, v159\n"
      "v_cvt_pk_bf16_f32 v220, v160, v161\n"
      "v_cvt_pk_bf16_f32 v221, v162, v163\n"
      "v_cvt_pk_bf16_f32 v222, v164, v165\n"
      "v_cvt_pk_bf16_f32 v223, v166, v167\n"
      "v_mul_f32 v152, s60, v32\n"
      "v_mul_f32 v153, s60, v33\n"
      "v_mul_f32 v154, s60, v34\n"
      "v_mul_f32 v155, s60, v35\n"
      "v_mul_f32 v156, s60, v36\n"
      "v_mul_f32 v157, s60, v37\n"
      "v_mul_f32 v158, s60, v38\n"
      "v_mul_f32 v159, s60, v39\n"
      "v_mul_f32 v160, s60, v40\n"
      "v_mul_f32 v161, s60, v41\n"
      "v_mul_f32 v162, s60, v42\n"
      "v_mul_f32 v163, s60, v43\n"
      "v_mul_f32 v164, s60, v44\n"
      "v_mul_f32 v165, s60, v45\n"
      "v_mul_f32 v166, s60, v46\n"
      "v_mul_f32 v167, s60, v47\n"
      "v_exp_f32_e64 v152, v152 clamp\n"
      "v_exp_f32_e64 v153, v153 clamp\n"
      "v_exp_f32_e64 v154, v154 clamp\n"
      "v_exp_f32_e64 v155, v155 clamp\n"
      "v_exp_f32_e64 v156, v156 clamp\n"
      "v_exp_f32_e64 v157, v157 clamp\n"
      "v_exp_f32_e64 v158, v158 clamp\n"
      "v_exp_f32_e64 v159, v159 clamp\n"
      "v_exp_f32_e64 v160, v160 clamp\n"
      "v_exp_f32_e64 v161, v161 clamp\n"
      "v_exp_f32_e64 v162, v162 clamp\n"
      "v_exp_f32_e64 v163, v163 clamp\n"
      "v_exp_f32_e64 v164, v164 clamp\n"
      "v_exp_f32_e64 v165, v165 clamp\n"
      "v_exp_f32_e64 v166, v166 clamp\n"
      "v_exp_f32_e64 v167, v167 clamp\n"
      "v_max_f32 v32, 0, v32\n"
      "v_max_f32 v33, 0, v33\n"
      "v_max_f32 v34, 0, v34\n"
      "v_max_f32 v35, 0, v35\n"
      "v_max_f32 v36, 0, v36\n"
      "v_max_f32 v37, 0, v37\n"
      "v_max_f32 v38, 0, v38\n"
      "v_max_f32 v39, 0, v39\n"
      "v_max_f32 v40, 0, v40\n"
      "v_max_f32 v41, 0, v41\n"
      "v_max_f32 v42, 0, v42\n"
      "v_max_f32 v43, 0, v43\n"
      "v_max_f32 v44, 0, v44\n"
      "v_max_f32 v45, 0, v45\n"
      "v_max_f32 v46, 0, v46\n"
      "v_max_f32 v47, 0, v47\n"
      "v_add_f32 v152, v152, v32\n"
      "v_add_f32 v153, v153, v33\n"
      "v_add_f32 v154, v154, v34\n"
      "v_add_f32 v155, v155, v35\n"
      "v_add_f32 v156, v156, v36\n"
      "v_add_f32 v157, v157, v37\n"
      "v_add_f32 v158, v158, v38\n"
      "v_add_f32 v159, v159, v39\n"
      "v_add_f32 v160, v160, v40\n"
      "v_add_f32 v161, v161, v41\n"
      "v_add_f32 v162, v162, v42\n"
      "v_add_f32 v163, v163, v43\n"
      "v_add_f32 v164, v164, v44\n"
      "v_add_f32 v165, v165, v45\n"
      "v_add_f32 v166, v166, v46\n"
      "v_add_f32 v167, v167, v47\n"
      "v_add_f32 v152, s68, v152\n"
      "v_add_f32 v153, s68, v153\n"
      "v_add_f32 v154, s68, v154\n"
      "v_add_f32 v155, s68, v155\n"
      "v_add_f32 v156, s68, v156\n"
      "v_add_f32 v157, s68, v157\n"
      "v_add_f32 v158, s68, v158\n"
      "v_add_f32 v159, s68, v159\n"
      "v_add_f32 v160, s68, v160\n"
      "v_add_f32 v161, s68, v161\n"
      "v_add_f32 v162, s68, v162\n"
      "v_add_f32 v163, s68, v163\n"
      "v_add_f32 v164, s68, v164\n"
      "v_add_f32 v165, s68, v165\n"
      "v_add_f32 v166, s68, v166\n"
      "v_add_f32 v167, s68, v167\n"
      "v_cvt_pk_bf16_f32 v224, v152, v153\n"
      "v_cvt_pk_bf16_f32 v225, v154, v155\n"
      "v_cvt_pk_bf16_f32 v226, v156, v157\n"
      "v_cvt_pk_bf16_f32 v227, v158, v159\n"
      "v_cvt_pk_bf16_f32 v228, v160, v161\n"
      "v_cvt_pk_bf16_f32 v229, v162, v163\n"
      "v_cvt_pk_bf16_f32 v230, v164, v165\n"
      "v_cvt_pk_bf16_f32 v231, v166, v167\n"
      "v_mov_b32 v9, v8\n"
      "s_nop 1\n"
      "v_permlane32_swap_b32 v9, v8\n"
      "v_add_f32 v8, v8, v9\n"
      "v_min_f32 v8, 0x41f00000, v8\n"
      "v_mul_f32 v8, s60, v8\n"
      "v_exp_f32 v8, v8\n"
      "s_nop 0\n"
      "v_add_f32 v9, 1.0, v8\n"
      "v_rcp_f32 v9, v9\n"
      "v_lshlrev_b32 v10, 2, v0\n"
      "s_mov_b32 exec_hi, 0\n"
      "buffer_store_dword v9, v10, s[16:19], s42 offen sc1\n"
      "s_mov_b32 exec_hi, -1\n"
      "v_add_u32 v10, 156672, v1\n"
      "v_mov_b32 v168, 160768\n"
      "s_mov_b32 s76, 0x400000\n"
      ".Llw_55_%=:\n"
      "ds_read_b128 v[170:173], v168\n"
      "ds_read_b128 v[152:155], v10 offset:0\n"
      "ds_read_b128 v[156:159], v10 offset:1024\n"
      "ds_read_b128 v[160:163], v10 offset:2048\n"
      "ds_read_b128 v[164:167], v10 offset:3072\n"
      "s_waitcnt lgkmcnt(0)\n"
      "v_min3_u32 v170, v170, v171, v172\n"
      "v_min_u32 v170, v170, v173\n"
      "v_cmp_le_u32 vcc, 4, v170\n"
      "s_cbranch_vccnz .Llwx_56_%=\n"
      "s_sub_u32 s76, s76, 1\n"
      "s_sleep 2\n"
      "s_cbranch_scc0 .Llw_55_%=\n"
      ".Llwx_56_%=:\n"
      "s_setprio 3\n"
      "v_mfma_f32_32x32x16_bf16 v[16:31], v[152:155], v[216:219], 0\n"
      "v_mfma_f32_32x32x16_bf16 v[32:47], v[156:159], v[220:223], 0\n"
      "v_mfma_f32_32x32x16_bf16 v[48:63], v[160:163], v[224:227], 0\n"
      "v_mfma_f32_32x32x16_bf16 v[64:79], v[164:167], v[228:231], 0\n"
      "s_nop 8\n"
      "v_add_f32 v168, s68, v24\n"
      "v_rcp_f32 v168, v168\n"
      "s_nop 0\n"
      "v_mul_f32 v168, v168, v8\n"
      "v_mul_f32 v16, v16, v168\n"
      "v_mul_f32 v17, v17, v168\n"
      "v_mul_f32 v18, v18, v168\n"
      "v_mul_f32 v19, v19, v168\n"
      "v_mul_f32 v20, v20, v168\n"
      "v_mul_f32 v21, v21, v168\n"
      "v_mul_f32 v22, v22, v168\n"
      "v_mul_f32 v23, v23, v168\n"
      "v_cvt_pk_bf16_f32 v232, v16, v17\n"
      "v_cvt_pk_bf16_f32 v233, v18, v19\n"
      "v_cvt_pk_bf16_f32 v234, v20, v21\n"
      "v_cvt_pk_bf16_f32 v235, v22, v23\n"
      "buffer_store_dwordx4 v[232:235], v1, s[16:19], s40 offen sc1\n"
      "v_add_f32 v170, s68, v32\n"
      "v_rcp_f32 v170, v170\n"
      "s_nop 0\n"
      "v_mul_f32 v170, v170, v8\n"
      "v_mul_f32 v40, v40, v170\n"
      "v_mul_f32 v41, v41, v170\n"
      "v_mul_f32 v42, v42, v170\n"
      "v_mul_f32 v43, v43, v170\n"
      "v_mul_f32 v44, v44, v170\n"
      "v_mul_f32 v45, v45, v170\n"
      "v_mul_f32 v46, v46, v170\n"
      "v_mul_f32 v47, v47, v170\n"
      "v_cvt_pk_bf16_f32 v236, v40, v41\n"
      "v_cvt_pk_bf16_f32 v237, v42, v43\n"
      "v_cvt_pk_bf16_f32 v238, v44, v45\n"
      "v_cvt_pk_bf16_f32 v239, v46, v47\n"
      "buffer_store_dwordx4 v[236:239], v1, s[16:19], s40 offen offset:1024 sc1\n"
      "v_add_f32 v172, s68, v56\n"
      "v_rcp_f32 v172, v172\n"
      "s_nop 0\n"
      "v_mul_f32 v172, v172, v8\n"
      "v_mul_f32 v48, v48, v172\n"
      "v_mul_f32 v49, v49, v172\n"
      "v_mul_f32 v50, v50, v172\n"
      "v_mul_f32 v51, v51, v172\n"
      "v_mul_f32 v52, v52, v172\n"
      "v_mul_f32 v53, v53, v172\n"
      "v_mul_f32 v54, v54, v172\n"
      "v_mul_f32 v55, v55, v172\n"
      "v_cvt_pk_bf16_f32 v240, v48, v49\n"
      "v_cvt_pk_bf16_f32 v241, v50, v51\n"
      "v_cvt_pk_bf16_f32 v242, v52, v53\n"
      "v_cvt_pk_bf16_f32 v243, v54, v55\n"
      "buffer_store_dwordx4 v[240:243], v1, s[16:19], s40 offen offset:2048 sc1\n"
      "v_add_f32 v174, s68, v64\n"
      "v_rcp_f32 v174, v174\n"
      "s_nop 0\n"
      "v_mul_f32 v174, v174, v8\n"
      "v_mul_f32 v72, v72, v174\n"
      "v_mul_f32 v73, v73, v174\n"
      "v_mul_f32 v74, v74, v174\n"
      "v_mul_f32 v75, v75, v174\n"
      "v_mul_f32 v76, v76, v174\n"
      "v_mul_f32 v77, v77, v174\n"
      "v_mul_f32 v78, v78, v174\n"
      "v_mul_f32 v79, v79, v174\n"
      "v_cvt_pk_bf16_f32 v244, v72, v73\n"
      "v_cvt_pk_bf16_f32 v245, v74, v75\n"
      "v_cvt_pk_bf16_f32 v246, v76, v77\n"
      "v_cvt_pk_bf16_f32 v247, v78, v79\n"
      "buffer_store_dwordx4 v[244:247], v1, s[16:19], s40 offen offset:3072 sc1\n"
      ".LPB_4_%=:\n"
      "s_lshr_b32 s62, s36, 1\n"
      "s_lshl3_add_u32 s62, s62, s98\n"
      "s_and_b32 s63, s36, 1\n"
      "s_lshl1_add_u32 s62, s62, s63\n"
      "s_and_b32 s63, s99, 1\n"
      "s_lshl1_add_u32 s62, s62, s63\n"
      "s_lshl_b32 s62, s62, 7\n"
      "s_add_u32 s43, s62, 11042816\n"
      "v_and_b32 v9, 31, v0\n"
      "v_lshrrev_b32 v8, 5, v0\n"
      "v_and_b32 v15, 7, v0\n"
      "v_xor_b32 v8, v8, v15\n"
      "v_lshlrev_b32 v8, 4, v8\n"
      "v_lshl_or_b32 v8, v9, 8, v8\n"
      "s_add_u32 s62, s96, 4096\n"
      "v_add_u32 v60, s62, v8\n"
      "v_xor_b32 v61, 32, v60\n"
      "v_xor_b32 v62, 64, v60\n"
      "v_xor_b32 v63, 96, v60\n"
      "v_add_u32 v56, 98304, v2\n"
      "v_add_u32 v57, 98304, v3\n"
      "s_waitcnt vmcnt(0)\n"
      "s_mov_b64 exec, 0xff\n"
      "buffer_atomic_swap_x2 v[12:13], v7, s[16:19], s45 offen sc1\n"
      "s_mov_b64 exec, -1\n"
      "s_lshl2_add_u32 s70, s99, 160832\n"
      "v_mov_b32 v240, s70\n"
      "v_mov_b32 v241, 2\n"
      "ds_write_b32 v240, v241\n"
      "s_cmp_lg_u32 s33, 0\n"
      "s_cbranch_scc1 .Lnp_58_%=\n"
      "s_sleep 8\n"
      ".Lnp_58_%=:\n"
      "s_mov_b64 exec, 15\n"
      "v_mov_b64 v[254:255], 0\n"
      "buffer_atomic_or_x2 v[254:255], v252, s[16:19], s54 offen sc0 sc1\n"
      "s_mov_b64 exec, -1\n"
      "v_mov_b32 v240, 160832\n"
      "s_mov_b32 s76, 0x400000\n"
      ".Llw_59_%=:\n"
      "ds_read_b128 v[242:245], v240\n"
      "s_waitcnt lgkmcnt(0)\n"
      "v_min3_u32 v242, v242, v243, v244\n"
      "v_min_u32 v242, v242, v245\n"
      "v_cmp_le_u32 vcc, 2, v242\n"
      "s_cbranch_vccnz .Llwx_60_%=\n"
      "s_sub_u32 s76, s76, 1\n"
      "s_sleep 2\n"
      "s_cbranch_scc0 .Llw_59_%=\n"
      ".Llwx_60_%=:\n"
      "s_waitcnt vmcnt(0)\n"
      "s_mov_b32 s76, 0x8000\n"
      "s_mov_b64 exec, 15\n"
      ".Lpl_61_%=:\n"
      "v_cmp_ne_u64 vcc, s[58:59], v[254:255]\n"
      "s_cbranch_vccz .Lpd_62_%=\n"
      "s_sub_u32 s76, s76, 1\n"
      "s_cbranch_scc1 .Lpd_62_%=\n"
      "s_sleep 2\n"
      "s_waitcnt vmcnt(0)\n"
      "v_mov_b64 v[254:255], 0\n"
      "buffer_atomic_or_x2 v[254:255], v252, s[16:19], s54 offen sc0 sc1\n"
      "s_waitcnt vmcnt(0)\n"
      "s_branch .Lpl_61_%=\n"
      ".Lpd_62_%=:\n"
      "s_mov_b64 exec, -1\n"
      "s_setprio 1\n"
      "s_add_u32 s41, s54, 262144\n"
      "s_add_u32 s44, s46, 8192\n"
      "buffer_load_dwordx4 v[88:91], v1, s[16:19], s44 offen offset:0 sc1\n"
      "buffer_load_dwordx4 v[92:95], v1, s[16:19], s44 offen offset:1024 sc1\n"
      "buffer_load_dwordx4 v[96:99], v1, s[16:19], s44 offen offset:2048 sc1\n"
      "buffer_load_dwordx4 v[100:103], v1, s[16:19], s44 offen offset:3072 sc1\n"
      "s_add_u32 s44, s46, 24576\n"
      "buffer_load_dwordx4 v[104:107], v1, s[16:19], s44 offen offset:0 sc1\n"
      "buffer_load_dwordx4 v[108:111], v1, s[16:19], s44 offen offset:1024 sc1\n"
      "buffer_load_dwordx4 v[112:115], v1, s[16:19], s44 offen offset:2048 sc1\n"
      "buffer_load_dwordx4 v[116:119], v1, s[16:19], s44 offen offset:3072 sc1\n"
      "s_add_u32 s44, s46, 12288\n"
      "buffer_load_dwordx4 v[120:123], v1, s[16:19], s44 offen offset:0 sc1\n"
      "buffer_load_dwordx4 v[124:127], v1, s[16:19], s44 offen offset:1024 sc1\n"
      "buffer_load_dwordx4 v[128:131], v1, s[16:19], s44 offen offset:2048 sc1\n"
      "buffer_load_dwordx4 v[132:135], v1, s[16:19], s44 offen offset:3072 sc1\n"
      "s_add_u32 s44, s48, 8192\n"
      "buffer_load_dwordx4 v[136:139], v1, s[16:19], s44 offen offset:0 sc1\n"
      "buffer_load_dwordx4 v[140:143], v1, s[16:19], s44 offen offset:1024 sc1\n"
      "buffer_load_dwordx4 v[144:147], v1, s[16:19], s44 offen offset:2048 sc1\n"
      "buffer_load_dwordx4 v[148:151], v1, s[16:19], s44 offen offset:3072 sc1\n"
      "s_setprio 1\n"
      "ds_read_b64_tr_b16 v[16:17], v2 offset:4096\n"
      "ds_read_b64_tr_b16 v[18:19], v2 offset:5120\n"
      "ds_read_b64_tr_b16 v[20:21], v3 offset:4096\n"
      "ds_read_b64_tr_b16 v[22:23], v3 offset:5120\n"
      "ds_read_b64_tr_b16 v[24:25], v2 offset:6144\n"
      "ds_read_b64_tr_b16 v[26:27], v2 offset:7168\n"
      "ds_read_b64_tr_b16 v[28:29], v3 offset:6144\n"
      "ds_read_b64_tr_b16 v[30:31], v3 offset:7168\n"
      "s_waitcnt lgkmcnt(4)\n"
      "s_waitcnt vmcnt(15)\n"
      "v_mfma_f32_32x32x16_bf16 v[152:167], v[16:19], v[88:91], 0\n"
      "ds_read_b64_tr_b16 v[32:33], v2 offset:8192\n"
      "ds_read_b64_tr_b16 v[34:35], v2 offset:9216\n"
      "v_mfma_f32_32x32x16_bf16 v[168:183], v[20:23], v[88:91], 0\n"
      "ds_read_b64_tr_b16 v[36:37], v3 offset:8192\n"
      "ds_read_b64_tr_b16 v[38:39], v3 offset:9216\n"
      "s_waitcnt lgkmcnt(4)\n"
      "s_waitcnt vmcnt(14)\n"
      "v_mfma_f32_32x32x16_bf16 v[152:167], v[24:27], v[92:95], v[152:167]\n"
      "ds_read_b64_tr_b16 v[40:41], v2 offset:10240\n"
      "ds_read_b64_tr_b16 v[42:43], v2 offset:11264\n"
      "v_mfma_f32_32x32x16_bf16 v[168:183], v[28:31], v[92:95], v[168:183]\n"
      "ds_read_b64_tr_b16 v[44:45], v3 offset:10240\n"
      "ds_read_b64_tr_b16 v[46:47], v3 offset:11264\n"
      "s_waitcnt lgkmcnt(4)\n"
      "s_waitcnt vmcnt(13)\n"
      "v_mfma_f32_32x32x16_bf16 v[152:167], v[32:35], v[96:99], v[152:167]\n"
      "ds_read_b64_tr_b16 v[48:49], v2 offset:16384\n"
      "ds_read_b64_tr_b16 v[50:51], v2 offset:17408\n"
      "v_mfma_f32_32x32x16_bf16 v[168:183], v[36:39], v[96:99], v[168:183]\n"
      "ds_read_b64_tr_b16 v[52:53], v3 offset:16384\n"
      "ds_read_b64_tr_b16 v[54:55], v3 offset:17408\n"
      "s_waitcnt lgkmcnt(4)\n"
      "s_waitcnt vmcnt(12)\n"
      "v_mfma_f32_32x32x16_bf16 v[152:167], v[40:43], v[100:103], v[152:167]\n"
      "ds_read_b64_tr_b16 v[216:217], v2 offset:18432\n"
      "ds_read_b64_tr_b16 v[218:219], v2 offset:19456\n"
      "v_mfma_f32_32x32x16_bf16 v[168:183], v[44:47], v[100:103], v[168:183]\n"
      "ds_read_b64_tr_b16 v[220:221], v3 offset:18432\n"
      "ds_read_b64_tr_b16 v[222:223], v3 offset:19456\n"
      "s_waitcnt lgkmcnt(4)\n"
      "s_waitcnt vmcnt(11)\n"
      "v_mfma_f32_32x32x16_bf16 v[152:167], v[48:51], v[104:107], v[152:167]\n"
      "ds_read_b64_tr_b16 v[224:225], v2 offset:20480\n"
      "ds_read_b64_tr_b16 v[226:227], v2 offset:21504\n"
      "v_mfma_f32_32x32x16_bf16 v[168:183], v[52:55], v[104:107], v[168:183]\n"
      "ds_read_b64_tr_b16 v[228:229], v3 offset:20480\n"
      "ds_read_b64_tr_b16 v[230:231], v3 offset:21504\n"
      "s_waitcnt lgkmcnt(4)\n"
      "s_waitcnt vmcnt(10)\n"
      "v_mfma_f32_32x32x16_bf16 v[152:167], v[216:219], v[108:111], v[152:167]\n"
      "ds_read_b64_tr_b16 v[232:233], v2 offset:22528\n"
      "ds_read_b64_tr_b16 v[234:235], v2 offset:23552\n"
      "v_mfma_f32_32x32x16_bf16 v[168:183], v[220:223], v[108:111], v[168:183]\n"
      "ds_read_b64_tr_b16 v[236:237], v3 offset:22528\n"
      "ds_read_b64_tr_b16 v[238:239], v3 offset:23552\n"
      "v_mov_b32 v240, 160848\n"
      "s_mov_b32 s76, 0x400000\n"
      ".Llw_63_%=:\n"
      "ds_read_b128 v[242:245], v240\n"
      "s_waitcnt lgkmcnt(0)\n"
      "v_min3_u32 v242, v242, v243, v244\n"
      "v_min_u32 v242, v242, v245\n"
      "v_cmp_le_u32 vcc, 1, v242\n"
      "s_cbranch_vccnz .Llwx_64_%=\n"
      "s_sub_u32 s76, s76, 1\n"
      "s_sleep 2\n"
      "s_cbranch_scc0 .Llw_63_%=\n"
      ".Llwx_64_%=:\n"
      "s_waitcnt lgkmcnt(4)\n"
      "s_waitcnt vmcnt(9)\n"
      "v_mfma_f32_32x32x16_bf16 v[152:167], v[224:227], v[112:115], v[152:167]\n"
      "ds_read_b64_tr_b16 v[80:81], v5 offset:0\n"
      "ds_read_b64_tr_b16 v[82:83], v5 offset:1024\n"
      "v_mfma_f32_32x32x16_bf16 v[168:183], v[228:231], v[112:115], v[168:183]\n"
      "ds_read_b64_tr_b16 v[84:85], v6 offset:0\n"
      "ds_read_b64_tr_b16 v[86:87], v6 offset:1024\n"
      "s_waitcnt lgkmcnt(4)\n"
      "s_waitcnt vmcnt(8)\n"
      "s_lshl2_add_u32 s70, s99, 160784\n"
      "v_mov_b32 v240, s70\n"
      "v_mov_b32 v241, 4\n"
      "ds_write_b32 v240, v241\n"
      "v_mfma_f32_32x32x16_bf16 v[152:167], v[232:235], v[116:119], v[152:167]\n"
      "ds_read_b64_tr_b16 v[64:65], v5 offset:2048\n"
      "ds_read_b64_tr_b16 v[66:67], v5 offset:3072\n"
      "v_mfma_f32_32x32x16_bf16 v[168:183], v[236:239], v[116:119], v[168:183]\n"
      "ds_read_b64_tr_b16 v[68:69], v6 offset:2048\n"
      "ds_read_b64_tr_b16 v[70:71], v6 offset:3072\n"
      "s_waitcnt lgkmcnt(4)\n"
      "s_waitcnt vmcnt(7)\n"
      "v_mfma_f32_32x32x16_bf16 v[152:167], v[80:83], v[120:123], v[152:167]\n"
      "ds_read_b64_tr_b16 v[72:73], v5 offset:12288\n"
      "ds_read_b64_tr_b16 v[74:75], v5 offset:13312\n"
      "v_mfma_f32_32x32x16_bf16 v[168:183], v[84:87], v[120:123], v[168:183]\n"
      "ds_read_b64_tr_b16 v[76:77], v6 offset:12288\n"
      "ds_read_b64_tr_b16 v[78:79], v6 offset:13312\n"
      "s_waitcnt lgkmcnt(4)\n"
      "s_waitcnt vmcnt(6)\n"
      "v_mfma_f32_32x32x16_bf16 v[152:167], v[64:67], v[124:127], v[152:167]\n"
      "ds_read_b64_tr_b16 v[80:81], v5 offset:14336\n"
      "ds_read_b64_tr_b16 v[82:83], v5 offset:15360\n"
      "v_mfma_f32_32x32x16_bf16 v[168:183], v[68:71], v[124:127], v[168:183]\n"
      "ds_read_b64_tr_b16 v[84:85], v6 offset:14336\n"
      "ds_read_b64_tr_b16 v[86:87], v6 offset:15360\n"
      "s_waitcnt lgkmcnt(4)\n"
      "s_waitcnt vmcnt(5)\n"
      "v_mfma_f32_32x32x16_bf16 v[152:167], v[72:75], v[128:131], v[152:167]\n"
      "v_mfma_f32_32x32x16_bf16 v[168:183], v[76:79], v[128:131], v[168:183]\n"
      "s_mov_b64 exec, 15\n"
      "v_mov_b64 v[10:11], 0\n"
      "buffer_atomic_or_x2 v[10:11], v252, s[16:19], s41 offen sc0 sc1\n"
      "s_mov_b64 exec, -1\n"
      "s_xor_b32 s70, s99, 1\n"
      "s_lshl2_add_u32 s70, s70, 160784\n"
      "v_mov_b32 v240, s70\n"
      "s_mov_b32 s76, 0x400000\n"
      ".Llo_65_%=:\n"
      "ds_read_b32 v241, v240\n"
      "s_waitcnt lgkmcnt(0)\n"
      "v_cmp_le_u32 vcc, 4, v241\n"
      "s_cbranch_vccnz .Llox_66_%=\n"
      "s_sub_u32 s76, s76, 1\n"
      "s_sleep 1\n"
      "s_cbranch_scc0 .Llo_65_%=\n"
      ".Llox_66_%=:\n"
      "s_waitcnt lgkmcnt(0)\n"
      "s_waitcnt vmcnt(4)\n"
      "v_mfma_f32_32x32x16_bf16 v[152:167], v[80:83], v[132:135], v[152:167]\n"
      "v_mfma_f32_32x32x16_bf16 v[168:183], v[84:87], v[132:135], v[168:183]\n"
      "s_setprio 3\n"
      "s_nop 9\n"
      "ds_write_b128 v60, v[152:155] offset:0\n"
      "ds_write_b128 v61, v[156:159] offset:0\n"
      "ds_write_b128 v62, v[160:163] offset:0\n"
      "ds_write_b128 v63, v[164:167] offset:0\n"
      "ds_write_b128 v60, v[168:171] offset:128\n"
      "ds_write_b128 v61, v[172:175] offset:128\n"
      "ds_write_b128 v62, v[176:179] offset:128\n"
      "ds_write_b128 v63, v[180:183] offset:128\n"
      "s_lshl2_add_u32 s70, s99, 160784\n"
      "v_mov_b32 v240, s70\n"
      "v_mov_b32 v241, 5\n"
      "ds_write_b32 v240, v241\n"
      "s_setprio 0\n"
      "s_add_u32 s44, s48, 24576\n"
      "buffer_load_dwordx4 v[88:91], v1, s[16:19], s44 offen offset:0 sc1\n"
      "buffer_load_dwordx4 v[92:95], v1, s[16:19], s44 offen offset:1024 sc1\n"
      "buffer_load_dwordx4 v[96:99], v1, s[16:19], s44 offen offset:2048 sc1\n"
      "buffer_load_dwordx4 v[100:103], v1, s[16:19], s44 offen offset:3072 sc1\n"
      "s_waitcnt vmcnt(4)\n"
      "s_mov_b32 s76, 0x8000\n"
      "s_mov_b64 exec, 15\n"
      ".Lpl_67_%=:\n"
      "v_cmp_ne_u64 vcc, s[58:59], v[10:11]\n"
      "s_cbranch_vccz .Lpd_68_%=\n"
      "s_sub_u32 s76, s76, 1\n"
      "s_cbranch_scc1 .Lpd_68_%=\n"
      "s_sleep 2\n"
      "s_waitcnt vmcnt(0)\n"
      "v_mov_b64 v[10:11], 0\n"
      "buffer_atomic_or_x2 v[10:11], v252, s[16:19], s41 offen sc0 sc1\n"
      "s_waitcnt vmcnt(0)\n"
      "s_branch .Lpl_67_%=\n"
      ".Lpd_68_%=:\n"
      "s_mov_b64 exec, -1\n"
      "v_and_b32 v15, 31, v0\n"
      "v_lshlrev_b32 v15, 2, v15\n"
      "buffer_load_dword v14, v15, s[16:19], s43 offen sc1\n"
      "s_add_u32 s44, s49, 0\n"
      "buffer_load_dwordx4 v[104:107], v1, s[16:19], s44 offen offset:0 sc1\n"
      "buffer_load_dwordx4 v[108:111], v1, s[16:19], s44 offen offset:1024 sc1\n"
      "v_mfma_f32_32x32x16_bf16 v[184:199], v[16:19], v[136:139], 0\n"
      "buffer_load_dwordx4 v[112:115], v1, s[16:19], s44 offen offset:2048 sc1\n"
      "v_mfma_f32_32x32x16_bf16 v[200:215], v[20:23], v[136:139], 0\n"
      "v_mfma_f32_32x32x16_bf16 v[184:199], v[24:27], v[140:143], v[184:199]\n"
      "buffer_load_dwordx4 v[116:119], v1, s[16:19], s44 offen offset:3072 sc1\n"
      "v_mfma_f32_32x32x16_bf16 v[200:215], v[28:31], v[140:143], v[200:215]\n"
      "v_mfma_f32_32x32x16_bf16 v[184:199], v[32:35], v[144:147], v[184:199]\n"
      "s_add_u32 s44, s49, 4096\n"
      "buffer_load_dwordx4 v[120:123], v1, s[16:19], s44 offen offset:0 sc1\n"
      "v_mfma_f32_32x32x16_bf16 v[200:215], v[36:39], v[144:147], v[200:215]\n"
      "v_mfma_f32_32x32x16_bf16 v[184:199], v[40:43], v[148:151], v[184:199]\n"
      "buffer_load_dwordx4 v[124:127], v1, s[16:19], s44 offen offset:1024 sc1\n"
      "v_mfma_f32_32x32x16_bf16 v[200:215], v[44:47], v[148:151], v[200:215]\n"
      "s_waitcnt vmcnt(10)\n"
      "v_mfma_f32_32x32x16_bf16 v[184:199], v[48:51], v[88:91], v[184:199]\n"
      "buffer_load_dwordx4 v[128:131], v1, s[16:19], s44 offen offset:2048 sc1\n"
      "v_mfma_f32_32x32x16_bf16 v[200:215], v[52:55], v[88:91], v[200:215]\n"
      "s_waitcnt vmcnt(10)\n"
      "v_mfma_f32_32x32x16_bf16 v[184:199], v[216:219], v[92:95], v[184:199]\n"
      "buffer_load_dwordx4 v[132:135], v1, s[16:19], s44 offen offset:3072 sc1\n"
      "v_mfma_f32_32x32x16_bf16 v[200:215], v[220:223], v[92:95], v[200:215]\n"
      "s_waitcnt vmcnt(10)\n"
      "v_mfma_f32_32x32x16_bf16 v[184:199], v[224:227], v[96:99], v[184:199]\n"
      "ds_read_b64_tr_b16 v[80:81], v5 offset:4096\n"
      "ds_read_b64_tr_b16 v[82:83], v5 offset:5120\n"
      "s_add_u32 s44, s47, 0\n"
      "buffer_load_dwordx4 v[136:139], v1, s[16:19], s44 offen offset:0 sc1\n"
      "v_mfma_f32_32x32x16_bf16 v[200:215], v[228:231], v[96:99], v[200:215]\n"
      "ds_read_b64_tr_b16 v[84:85], v6 offset:4096\n"
      "ds_read_b64_tr_b16 v[86:87], v6 offset:5120\n"
      "s_waitcnt vmcnt(10)\n"
      "v_mfma_f32_32x32x16_bf16 v[184:199], v[232:235], v[100:103], v[184:199]\n"
      "ds_read_b64_tr_b16 v[64:65], v5 offset:6144\n"
      "ds_read_b64_tr_b16 v[66:67], v5 offset:7168\n"
      "buffer_load_dwordx4 v[140:143], v1, s[16:19], s44 offen offset:1024 sc1\n"
      "v_mfma_f32_32x32x16_bf16 v[200:215], v[236:239], v[100:103], v[200:215]\n"
      "ds_read_b64_tr_b16 v[68:69], v6 offset:6144\n"
      "ds_read_b64_tr_b16 v[70:71], v6 offset:7168\n"
      "s_waitcnt lgkmcnt(4)\n"
      "s_waitcnt vmcnt(9)\n"
      "v_mfma_f32_32x32x16_bf16 v[184:199], v[80:83], v[104:107], v[184:199]\n"
      "ds_read_b64_tr_b16 v[72:73], v5 offset:8192\n"
      "ds_read_b64_tr_b16 v[74:75], v5 offset:9216\n"
      "buffer_load_dwordx4 v[144:147], v1, s[16:19], s44 offen offset:2048 sc1\n"
      "v_mfma_f32_32x32x16_bf16 v[200:215], v[84:87], v[104:107], v[200:215]\n"
      "ds_read_b64_tr_b16 v[76:77], v6 offset:8192\n"
      "ds_read_b64_tr_b16 v[78:79], v6 offset:9216\n"
      "s_waitcnt lgkmcnt(4)\n"
      "s_waitcnt vmcnt(9)\n"
      "v_mfma_f32_32x32x16_bf16 v[184:199], v[64:67], v[108:111], v[184:199]\n"
      "ds_read_b64_tr_b16 v[80:81], v5 offset:10240\n"
      "ds_read_b64_tr_b16 v[82:83], v5 offset:11264\n"
      "buffer_load_dwordx4 v[148:151], v1, s[16:19], s44 offen offset:3072 sc1\n"
      "v_mfma_f32_32x32x16_bf16 v[200:215], v[68:71], v[108:111], v[200:215]\n"
      "ds_read_b64_tr_b16 v[84:85], v6 offset:10240\n"
      "ds_read_b64_tr_b16 v[86:87], v6 offset:11264\n"
      "s_waitcnt lgkmcnt(4)\n"
      "s_waitcnt vmcnt(9)\n"
      "v_mfma_f32_32x32x16_bf16 v[184:199], v[72:75], v[112:115], v[184:199]\n"
      "ds_read_b64_tr_b16 v[64:65], v5 offset:16384\n"
      "ds_read_b64_tr_b16 v[66:67], v5 offset:17408\n"
      "s_add_u32 s44, s47, 4096\n"
      "buffer_load_dwordx4 v[88:91], v1, s[16:19], s44 offen offset:0 sc1\n"
      "v_mfma_f32_32x32x16_bf16 v[200:215], v[76:79], v[112:115], v[200:215]\n"
      "ds_read_b64_tr_b16 v[68:69], v6 offset:16384\n"
      "ds_read_b64_tr_b16 v[70:71], v6 offset:17408\n"
      "s_waitcnt lgkmcnt(4)\n"
      "s_waitcnt vmcnt(9)\n"
      "v_mfma_f32_32x32x16_bf16 v[184:199], v[80:83], v[116:119], v[184:199]\n"
      "ds_read_b64_tr_b16 v[72:73], v5 offset:18432\n"
      "ds_read_b64_tr_b16 v[74:75], v5 offset:19456\n"
      "buffer_load_dwordx4 v[92:95], v1, s[16:19], s44 offen offset:1024 sc1\n"
      "v_mfma_f32_32x32x16_bf16 v[200:215], v[84:87], v[116:119], v[200:215]\n"
      "ds_read_b64_tr_b16 v[76:77], v6 offset:18432\n"
      "ds_read_b64_tr_b16 v[78:79], v6 offset:19456\n"
      "s_waitcnt lgkmcnt(4)\n"
      "s_waitcnt vmcnt(9)\n"
      "v_mfma_f32_32x32x16_bf16 v[184:199], v[64:67], v[120:123], v[184:199]\n"
      "ds_read_b64_tr_b16 v[80:81], v5 offset:20480\n"
      "ds_read_b64_tr_b16 v[82:83], v5 offset:21504\n"
      "buffer_load_dwordx4 v[96:99], v1, s[16:19], s44 offen offset:2048 sc1\n"
      "v_mfma_f32_32x32x16_bf16 v[200:215], v[68:71], v[120:123], v[200:215]\n"
      "ds_read_b64_tr_b16 v[84:85], v6 offset:20480\n"
      "ds_read_b64_tr_b16 v[86:87], v6 offset:21504\n"
      "s_waitcnt lgkmcnt(4)\n"
      "s_waitcnt vmcnt(9)\n"
      "v_mfma_f32_32x32x16_bf16 v[184:199], v[72:75], v[124:127], v[184:199]\n"
      "ds_read_b64_tr_b16 v[64:65], v5 offset:22528\n"
      "ds_read_b64_tr_b16 v[66:67], v5 offset:23552\n"
      "buffer_load_dwordx4 v[100:103], v1, s[16:19], s44 offen offset:3072 sc1\n"
      "v_mfma_f32_32x32x16_bf16 v[200:215], v[76:79], v[124:127], v[200:215]\n"
      "ds_read_b64_tr_b16 v[68:69], v6 offset:22528\n"
      "ds_read_b64_tr_b16 v[70:71], v6 offset:23552\n"
      "v_mov_b32 v240, 160816\n"
      "s_mov_b32 s76, 0x400000\n"
      ".Llw_69_%=:\n"
      "ds_read_b128 v[242:245], v240\n"
      "s_waitcnt lgkmcnt(0)\n"
      "v_min3_u32 v242, v242, v243, v244\n"
      "v_min_u32 v242, v242, v245\n"
      "v_cmp_le_u32 vcc, 4, v242\n"
      "s_cbranch_vccnz .Llwx_70_%=\n"
      "s_sub_u32 s76, s76, 1\n"
      "s_sleep 2\n"
      "s_cbranch_scc0 .Llw_69_%=\n"
      ".Llwx_70_%=:\n"
      "s_waitcnt lgkmcnt(4)\n"
      "s_waitcnt vmcnt(9)\n"
      "v_mfma_f32_32x32x16_bf16 v[184:199], v[80:83], v[128:131], v[184:199]\n"
      "ds_read_b64_tr_b16 v[72:73], v56 offset:0\n"
      "ds_read_b64_tr_b16 v[74:75], v56 offset:1024\n"
      "v_mfma_f32_32x32x16_bf16 v[200:215], v[84:87], v[128:131], v[200:215]\n"
      "ds_read_b64_tr_b16 v[76:77], v57 offset:0\n"
      "ds_read_b64_tr_b16 v[78:79], v57 offset:1024\n"
      "s_waitcnt lgkmcnt(4)\n"
      "s_waitcnt vmcnt(8)\n"
      "v_mfma_f32_32x32x16_bf16 v[184:199], v[64:67], v[132:135], v[184:199]\n"
      "ds_read_b64_tr_b16 v[80:81], v56 offset:2048\n"
      "ds_read_b64_tr_b16 v[82:83], v56 offset:3072\n"
      "v_mfma_f32_32x32x16_bf16 v[200:215], v[68:71], v[132:135], v[200:215]\n"
      "ds_read_b64_tr_b16 v[84:85], v57 offset:2048\n"
      "ds_read_b64_tr_b16 v[86:87], v57 offset:3072\n"
      "s_waitcnt lgkmcnt(4)\n"
      "s_waitcnt vmcnt(7)\n"
      "v_mfma_f32_32x32x16_bf16 v[184:199], v[72:75], v[136:139], v[184:199]\n"
      "ds_read_b64_tr_b16 v[64:65], v56 offset:4096\n"
      "ds_read_b64_tr_b16 v[66:67], v56 offset:5120\n"
      "v_mfma_f32_32x32x16_bf16 v[200:215], v[76:79], v[136:139], v[200:215]\n"
      "ds_read_b64_tr_b16 v[68:69], v57 offset:4096\n"
      "ds_read_b64_tr_b16 v[70:71], v57 offset:5120\n"
      "s_waitcnt lgkmcnt(4)\n"
      "s_waitcnt vmcnt(6)\n"
      "v_mfma_f32_32x32x16_bf16 v[184:199], v[80:83], v[140:143], v[184:199]\n"
      "ds_read_b64_tr_b16 v[72:73], v56 offset:6144\n"
      "ds_read_b64_tr_b16 v[74:75], v56 offset:7168\n"
      "v_mfma_f32_32x32x16_bf16 v[200:215], v[84:87], v[140:143], v[200:215]\n"
      "ds_read_b64_tr_b16 v[76:77], v57 offset:6144\n"
      "ds_read_b64_tr_b16 v[78:79], v57 offset:7168\n"
      "s_waitcnt lgkmcnt(4)\n"
      "s_waitcnt vmcnt(5)\n"
      "v_mfma_f32_32x32x16_bf16 v[184:199], v[64:67], v[144:147], v[184:199]\n"
      "ds_read_b64_tr_b16 v[80:81], v56 offset:8192\n"
      "ds_read_b64_tr_b16 v[82:83], v56 offset:9216\n"
      "v_mfma_f32_32x32x16_bf16 v[200:215], v[68:71], v[144:147], v[200:215]\n"
      "ds_read_b64_tr_b16 v[84:85], v57 offset:8192\n"
      "ds_read_b64_tr_b16 v[86:87], v57 offset:9216\n"
      "s_waitcnt lgkmcnt(4)\n"
      "s_waitcnt vmcnt(4)\n"
      "v_mfma_f32_32x32x16_bf16 v[184:199], v[72:75], v[148:151], v[184:199]\n"
      "ds_read_b64_tr_b16 v[64:65], v56 offset:10240\n"
      "ds_read_b64_tr_b16 v[66:67], v56 offset:11264\n"
      "v_mfma_f32_32x32x16_bf16 v[200:215], v[76:79], v[148:151], v[200:215]\n"
      "ds_read_b64_tr_b16 v[68:69], v57 offset:10240\n"
      "ds_read_b64_tr_b16 v[70:71], v57 offset:11264\n"
      "s_waitcnt lgkmcnt(4)\n"
      "s_waitcnt vmcnt(3)\n"
      "v_mfma_f32_32x32x16_bf16 v[184:199], v[80:83], v[88:91], v[184:199]\n"
      "ds_read_b64_tr_b16 v[72:73], v2 offset:0\n"
      "ds_read_b64_tr_b16 v[74:75], v2 offset:1024\n"
      "v_mfma_f32_32x32x16_bf16 v[200:215], v[84:87], v[88:91], v[200:215]\n"
      "ds_read_b64_tr_b16 v[76:77], v3 offset:0\n"
      "ds_read_b64_tr_b16 v[78:79], v3 offset:1024\n"
      "s_waitcnt lgkmcnt(4)\n"
      "s_waitcnt vmcnt(2)\n"
      "v_mfma_f32_32x32x16_bf16 v[184:199], v[64:67], v[92:95], v[184:199]\n"
      "ds_read_b64_tr_b16 v[80:81], v2 offset:2048\n"
      "ds_read_b64_tr_b16 v[82:83], v2 offset:3072\n"
      "v_mfma_f32_32x32x16_bf16 v[200:215], v[68:71], v[92:95], v[200:215]\n"
      "ds_read_b64_tr_b16 v[84:85], v3 offset:2048\n"
      "ds_read_b64_tr_b16 v[86:87], v3 offset:3072\n"
      "s_lshl_b32 s62, s99, 7\n"
      "s_add_u32 s62, s62, 161024\n"
      "v_add_u32 v15, s62, v15\n"
      "ds_write_b32 v15, v14\n"
      "s_waitcnt lgkmcnt(4)\n"
      "s_waitcnt vmcnt(1)\n"
      "v_mfma_f32_32x32x16_bf16 v[184:199], v[72:75], v[96:99], v[184:199]\n"
      "v_mfma_f32_32x32x16_bf16 v[200:215], v[76:79], v[96:99], v[200:215]\n"
      "s_waitcnt lgkmcnt(0)\n"
      "s_waitcnt vmcnt(0)\n"
      "v_mfma_f32_32x32x16_bf16 v[184:199], v[80:83], v[100:103], v[184:199]\n"
      "v_mfma_f32_32x32x16_bf16 v[200:215], v[84:87], v[100:103], v[200:215]\n"
      "s_nop 10\n"
      "ds_write_b128 v60, v[184:187] offset:0\n"
      "ds_write_b128 v61, v[188:191] offset:0\n"
      "ds_write_b128 v62, v[192:195] offset:0\n"
      "ds_write_b128 v63, v[196:199] offset:0\n"
      "ds_write_b128 v60, v[200:203] offset:128\n"
      "ds_write_b128 v61, v[204:207] offset:128\n"
      "ds_write_b128 v62, v[208:211] offset:128\n"
      "ds_write_b128 v63, v[212:215] offset:128\n"
      "s_waitcnt lgkmcnt(0)\n"
      "s_barrier\n"
      "s_branch .Lend_57_%=\n"
      ".Llz_47_%=:\n"
      "v_mov_b32 v90, 160784\n"
      "s_mov_b32 s76, 0x400000\n"
      ".Llw_49_%=:\n"
      "ds_read_b128 v[92:95], v90\n"
      "s_waitcnt lgkmcnt(0)\n"
      "v_min3_u32 v92, v92, v93, v94\n"
      "v_min_u32 v92, v92, v95\n"
      "v_cmp_le_u32 vcc, 1, v92\n"
      "s_cbranch_vccnz .Llwx_50_%=\n"
      "s_sub_u32 s76, s76, 1\n"
      "s_cbranch_scc0 .Llw_49_%=\n"
      ".Llwx_50_%=:\n"
      "s_branch .Llzr_48_%=\n"
      ".Llz_51_%=:\n"
      "v_mov_b32 v90, 160784\n"
      "s_mov_b32 s76, 0x400000\n"
      ".Llw_53_%=:\n"
      "ds_read_b128 v[92:95], v90\n"
      "s_waitcnt lgkmcnt(0)\n"
      "v_min3_u32 v92, v92, v93, v94\n"
      "v_min_u32 v92, v92, v95\n"
      "v_cmp_le_u32 vcc, 2, v92\n"
      "s_cbranch_vccnz .Llwx_54_%=\n"
      "s_sub_u32 s76, s76, 1\n"
      "s_cbranch_scc0 .Llw_53_%=\n"
      ".Llwx_54_%=:\n"
      "s_branch .Llzr_52_%=\n"
      ".LPA_3_%=:\n"
      "s_mov_b32 s76, 0x8000\n"
      "s_mov_b64 exec, 15\n"
      ".Lpl_71_%=:\n"
      "v_cmp_ne_u64 vcc, s[58:59], v[254:255]\n"
      "s_cbranch_vccz .Lpd_72_%=\n"
      "s_sub_u32 s76, s76, 1\n"
      "s_cbranch_scc1 .Lpd_72_%=\n"
      "s_sleep 2\n"
      "s_waitcnt vmcnt(0)\n"
      "v_mov_b64 v[254:255], 0\n"
      "buffer_atomic_or_x2 v[254:255], v252, s[16:19], s54 offen sc0 sc1\n"
      "s_waitcnt vmcnt(0)\n"
      "s_branch .Lpl_71_%=\n"
      ".Lpd_72_%=:\n"
      "s_mov_b64 exec, -1\n"
      ".LPA2_5_%=:\n"
      "s_setprio 2\n"
      "s_add_u32 s41, s54, 262144\n"
      "s_add_u32 s44, s46, 0\n"
      "buffer_load_dwordx4 v[88:91], v1, s[16:19], s44 offen offset:0 sc1\n"
      "buffer_load_dwordx4 v[92:95], v1, s[16:19], s44 offen offset:1024 sc1\n"
      "buffer_load_dwordx4 v[96:99], v1, s[16:19], s44 offen offset:2048 sc1\n"
      "buffer_load_dwordx4 v[100:103], v1, s[16:19], s44 offen offset:3072 sc1\n"
      "s_add_u32 s44, s46, 4096\n"
      "buffer_load_dwordx4 v[104:107], v1, s[16:19], s44 offen offset:0 sc1\n"
      "buffer_load_dwordx4 v[108:111], v1, s[16:19], s44 offen offset:1024 sc1\n"
      "buffer_load_dwordx4 v[112:115], v1, s[16:19], s44 offen offset:2048 sc1\n"
      "buffer_load_dwordx4 v[116:119], v1, s[16:19], s44 offen offset:3072 sc1\n"
      "s_add_u32 s44, s46, 16384\n"
      "buffer_load_dwordx4 v[120:123], v1, s[16:19], s44 offen offset:0 sc1\n"
      "buffer_load_dwordx4 v[124:127], v1, s[16:19], s44 offen offset:1024 sc1\n"
      "s_cmp_eq_u32 s53, 0\n"
      "s_cbranch_scc1 .Lnpd_73_%=\n"
      "s_waitcnt vmcnt(10)\n"
      "s_mov_b64 exec, 0xff\n"
      "buffer_atomic_swap_x2 v[12:13], v7, s[16:19], s45 offen sc1\n"
      "s_mov_b64 exec, -1\n"
      ".Lnpd_73_%=:\n"
      "buffer_load_dwordx4 v[128:131], v1, s[16:19], s44 offen offset:2048 sc1\n"
      "buffer_load_dwordx4 v[132:135], v1, s[16:19], s44 offen offset:3072 sc1\n"
      "s_add_u32 s44, s46, 20480\n"
      "buffer_load_dwordx4 v[136:139], v1, s[16:19], s44 offen offset:0 sc1\n"
      "buffer_load_dwordx4 v[140:143], v1, s[16:19], s44 offen offset:1024 sc1\n"
      "buffer_load_dwordx4 v[144:147], v1, s[16:19], s44 offen offset:2048 sc1\n"
      "buffer_load_dwordx4 v[148:151], v1, s[16:19], s44 offen offset:3072 sc1\n"
      "v_mov_b32 v18, 160832\n"
      "s_mov_b32 s76, 0x400000\n"
      ".Llw_74_%=:\n"
      "ds_read_b128 v[248:251], v18\n"
      "s_waitcnt lgkmcnt(0)\n"
      "v_min3_u32 v248, v248, v249, v250\n"
      "v_min_u32 v248, v248, v251\n"
      "v_cmp_le_u32 vcc, 1, v248\n"
      "s_cbranch_vccnz .Llwx_75_%=\n"
      "s_sub_u32 s76, s76, 1\n"
      "s_sleep 2\n"
      "s_cbranch_scc0 .Llw_74_%=\n"
      ".Llwx_75_%=:\n"
      "ds_read_b64_tr_b16 v[184:185], v5 offset:0\n"
      "ds_read_b64_tr_b16 v[186:187], v5 offset:1024\n"
      "ds_read_b64_tr_b16 v[188:189], v6 offset:0\n"
      "ds_read_b64_tr_b16 v[190:191], v6 offset:1024\n"
      "ds_read_b64_tr_b16 v[192:193], v5 offset:2048\n"
      "ds_read_b64_tr_b16 v[194:195], v5 offset:3072\n"
      "ds_read_b64_tr_b16 v[196:197], v6 offset:2048\n"
      "ds_read_b64_tr_b16 v[198:199], v6 offset:3072\n"
      "v_add_u32 v56, 7168, v4\n"
      "v_add_u32 v57, 14336, v4\n"
      "v_add_u32 v58, 21504, v4\n"
      "s_setprio 2\n"
      "s_waitcnt lgkmcnt(4)\n"
      "s_waitcnt vmcnt(15)\n"
      "v_mfma_f32_32x32x16_bf16 v[152:167], v[184:187], v[88:91], 0\n"
      "ds_read_b64_tr_b16 v[200:201], v5 offset:4096\n"
      "ds_read_b64_tr_b16 v[202:203], v5 offset:5120\n"
      "v_mov_b32 v18, 160784\n"
      "s_mov_b32 s76, 0x400000\n"
      ".Llw_76_%=:\n"
      "ds_read_b128 v[248:251], v18\n"
      "s_waitcnt lgkmcnt(0)\n"
      "v_min3_u32 v248, v248, v249, v250\n"
      "v_min_u32 v248, v248, v251\n"
      "v_cmp_le_u32 vcc, 3, v248\n"
      "s_cbranch_vccnz .Llwx_77_%=\n"
      "s_sub_u32 s76, s76, 1\n"
      "s_sleep 2\n"
      "s_cbranch_scc0 .Llw_76_%=\n"
      ".Llwx_77_%=:\n"
      "s_add_u32 m0, s96, 49152\n"
      "s_nop 0\n"
      "buffer_load_dwordx4 v4, s[8:11], s28 offen offset:0 lds\n"
      "buffer_load_dwordx4 v56, s[8:11], s28 offen offset:1024 lds\n"
      "buffer_load_dwordx4 v57, s[8:11], s28 offen offset:2048 lds\n"
      "buffer_load_dwordx4 v58, s[8:11], s28 offen offset:3072 lds\n"
      "v_mfma_f32_32x32x16_bf16 v[168:183], v[188:191], v[88:91], 0\n"
      "ds_read_b64_tr_b16 v[204:205], v6 offset:4096\n"
      "ds_read_b64_tr_b16 v[206:207], v6 offset:5120\n"
      "s_waitcnt lgkmcnt(4)\n"
      "s_waitcnt vmcnt(18)\n"
      "v_mfma_f32_32x32x16_bf16 v[152:167], v[192:195], v[92:95], v[152:167]\n"
      "ds_read_b64_tr_b16 v[208:209], v5 offset:6144\n"
      "ds_read_b64_tr_b16 v[210:211], v5 offset:7168\n"
      "s_add_u32 m0, s96, 53248\n"
      "s_nop 0\n"
      "buffer_load_dwordx4 v4, s[12:15], s29 offen offset:0 lds\n"
      "buffer_load_dwordx4 v56, s[12:15], s29 offen offset:1024 lds\n"
      "buffer_load_dwordx4 v57, s[12:15], s29 offen offset:2048 lds\n"
      "buffer_load_dwordx4 v58, s[12:15], s29 offen offset:3072 lds\n"
      "v_mfma_f32_32x32x16_bf16 v[168:183], v[196:199], v[92:95], v[168:183]\n"
      "ds_read_b64_tr_b16 v[212:213], v6 offset:6144\n"
      "ds_read_b64_tr_b16 v[214:215], v6 offset:7168\n"
      "s_waitcnt lgkmcnt(4)\n"
      "s_waitcnt vmcnt(21)\n"
      "v_mfma_f32_32x32x16_bf16 v[152:167], v[200:203], v[96:99], v[152:167]\n"
      "ds_read_b64_tr_b16 v[216:217], v5 offset:8192\n"
      "ds_read_b64_tr_b16 v[218:219], v5 offset:9216\n"
      "s_add_u32 m0, s96, 57344\n"
      "s_nop 0\n"
      "buffer_load_dwordx4 v4, s[12:15], s30 offen offset:0 lds\n"
      "buffer_load_dwordx4 v56, s[12:15], s30 offen offset:1024 lds\n"
      "buffer_load_dwordx4 v57, s[12:15], s30 offen offset:2048 lds\n"
      "buffer_load_dwordx4 v58, s[12:15], s30 offen offset:3072 lds\n"
      "v_mfma_f32_32x32x16_bf16 v[168:183], v[204:207], v[96:99], v[168:183]\n"
      "ds_read_b64_tr_b16 v[220:221], v6 offset:8192\n"
      "ds_read_b64_tr_b16 v[222:223], v6 offset:9216\n"
      "s_waitcnt lgkmcnt(4)\n"
      "s_waitcnt vmcnt(24)\n"
      "v_mfma_f32_32x32x16_bf16 v[152:167], v[208:211], v[100:103], v[152:167]\n"
      "ds_read_b64_tr_b16 v[224:225], v5 offset:10240\n"
      "ds_read_b64_tr_b16 v[226:227], v5 offset:11264\n"
      "s_mov_b64 exec, 15\n"
      "v_mov_b64 v[10:11], 0\n"
      "buffer_atomic_or_x2 v[10:11], v252, s[16:19], s41 offen sc0 sc1\n"
      "s_mov_b64 exec, -1\n"
      "v_mfma_f32_32x32x16_bf16 v[168:183], v[212:215], v[100:103], v[168:183]\n"
      "ds_read_b64_tr_b16 v[228:229], v6 offset:10240\n"
      "ds_read_b64_tr_b16 v[230:231], v6 offset:11264\n"
      "s_waitcnt lgkmcnt(4)\n"
      "s_waitcnt vmcnt(23)\n"
      "v_mfma_f32_32x32x16_bf16 v[152:167], v[216:219], v[104:107], v[152:167]\n"
      "ds_read_b64_tr_b16 v[232:233], v2 offset:0\n"
      "ds_read_b64_tr_b16 v[234:235], v2 offset:1024\n"
      "v_mfma_f32_32x32x16_bf16 v[168:183], v[220:223], v[104:107], v[168:183]\n"
      "ds_read_b64_tr_b16 v[236:237], v3 offset:0\n"
      "ds_read_b64_tr_b16 v[238:239], v3 offset:1024\n"
      "s_waitcnt lgkmcnt(4)\n"
      "s_waitcnt vmcnt(22)\n"
      "v_mfma_f32_32x32x16_bf16 v[152:167], v[224:227], v[108:111], v[152:167]\n"
      "ds_read_b64_tr_b16 v[240:241], v2 offset:2048\n"
      "ds_read_b64_tr_b16 v[242:243], v2 offset:3072\n"
      "v_mfma_f32_32x32x16_bf16 v[168:183], v[228:231], v[108:111], v[168:183]\n"
      "ds_read_b64_tr_b16 v[244:245], v3 offset:2048\n"
      "ds_read_b64_tr_b16 v[246:247], v3 offset:3072\n"
      "s_waitcnt lgkmcnt(4)\n"
      "s_waitcnt vmcnt(21)\n"
      "v_mfma_f32_32x32x16_bf16 v[152:167], v[232:235], v[112:115], v[152:167]\n"
      "ds_read_b64_tr_b16 v[80:81], v5 offset:12288\n"
      "ds_read_b64_tr_b16 v[82:83], v5 offset:13312\n"
      "v_mfma_f32_32x32x16_bf16 v[168:183], v[236:239], v[112:115], v[168:183]\n"
      "ds_read_b64_tr_b16 v[84:85], v6 offset:12288\n"
      "ds_read_b64_tr_b16 v[86:87], v6 offset:13312\n"
      "s_waitcnt lgkmcnt(4)\n"
      "s_waitcnt vmcnt(20)\n"
      "s_lshl2_add_u32 s70, s99, 160816\n"
      "v_mov_b32 v18, s70\n"
      "v_mov_b32 v19, 1\n"
      "ds_write_b32 v18, v19\n"
      "v_mfma_f32_32x32x16_bf16 v[152:167], v[240:243], v[116:119], v[152:167]\n"
      "ds_read_b64_tr_b16 v[64:65], v5 offset:14336\n"
      "ds_read_b64_tr_b16 v[66:67], v5 offset:15360\n"
      "v_mfma_f32_32x32x16_bf16 v[168:183], v[244:247], v[116:119], v[168:183]\n"
      "ds_read_b64_tr_b16 v[68:69], v6 offset:14336\n"
      "ds_read_b64_tr_b16 v[70:71], v6 offset:15360\n"
      "s_waitcnt lgkmcnt(4)\n"
      "s_waitcnt vmcnt(19)\n"
      "v_mfma_f32_32x32x16_bf16 v[152:167], v[80:83], v[120:123], v[152:167]\n"
      "ds_read_b64_tr_b16 v[72:73], v5 offset:16384\n"
      "ds_read_b64_tr_b16 v[74:75], v5 offset:17408\n"
      "v_mfma_f32_32x32x16_bf16 v[168:183], v[84:87], v[120:123], v[168:183]\n"
      "ds_read_b64_tr_b16 v[76:77], v6 offset:16384\n"
      "ds_read_b64_tr_b16 v[78:79], v6 offset:17408\n"
      "s_waitcnt lgkmcnt(4)\n"
      "s_waitcnt vmcnt(18)\n"
      "v_mfma_f32_32x32x16_bf16 v[152:167], v[64:67], v[124:127], v[152:167]\n"
      "ds_read_b64_tr_b16 v[80:81], v5 offset:18432\n"
      "ds_read_b64_tr_b16 v[82:83], v5 offset:19456\n"
      "v_mfma_f32_32x32x16_bf16 v[168:183], v[68:71], v[124:127], v[168:183]\n"
      "ds_read_b64_tr_b16 v[84:85], v6 offset:18432\n"
      "ds_read_b64_tr_b16 v[86:87], v6 offset:19456\n"
      "s_waitcnt lgkmcnt(4)\n"
      "s_waitcnt vmcnt(17)\n"
      "v_mfma_f32_32x32x16_bf16 v[152:167], v[72:75], v[128:131], v[152:167]\n"
      "ds_read_b64_tr_b16 v[64:65], v5 offset:20480\n"
      "ds_read_b64_tr_b16 v[66:67], v5 offset:21504\n"
      "v_mfma_f32_32x32x16_bf16 v[168:183], v[76:79], v[128:131], v[168:183]\n"
      "ds_read_b64_tr_b16 v[68:69], v6 offset:20480\n"
      "ds_read_b64_tr_b16 v[70:71], v6 offset:21504\n"
      "s_waitcnt lgkmcnt(4)\n"
      "s_waitcnt vmcnt(16)\n"
      "v_mfma_f32_32x32x16_bf16 v[152:167], v[80:83], v[132:135], v[152:167]\n"
      "ds_read_b64_tr_b16 v[72:73], v5 offset:22528\n"
      "ds_read_b64_tr_b16 v[74:75], v5 offset:23552\n"
      "v_mfma_f32_32x32x16_bf16 v[168:183], v[84:87], v[132:135], v[168:183]\n"
      "ds_read_b64_tr_b16 v[76:77], v6 offset:22528\n"
      "ds_read_b64_tr_b16 v[78:79], v6 offset:23552\n"
      "s_waitcnt lgkmcnt(4)\n"
      "s_waitcnt vmcnt(15)\n"
      "v_mfma_f32_32x32x16_bf16 v[152:167], v[64:67], v[136:139], v[152:167]\n"
      "ds_read_b64_tr_b16 v[80:81], v2 offset:12288\n"
      "ds_read_b64_tr_b16 v[82:83], v2 offset:13312\n"
      "v_mfma_f32_32x32x16_bf16 v[168:183], v[68:71], v[136:139], v[168:183]\n"
      "ds_read_b64_tr_b16 v[84:85], v3 offset:12288\n"
      "ds_read_b64_tr_b16 v[86:87], v3 offset:13312\n"
      "s_waitcnt lgkmcnt(4)\n"
      "s_waitcnt vmcnt(14)\n"
      "v_mfma_f32_32x32x16_bf16 v[152:167], v[72:75], v[140:143], v[152:167]\n"
      "ds_read_b64_tr_b16 v[64:65], v2 offset:14336\n"
      "ds_read_b64_tr_b16 v[66:67], v2 offset:15360\n"
      "v_mfma_f32_32x32x16_bf16 v[168:183], v[76:79], v[140:143], v[168:183]\n"
      "ds_read_b64_tr_b16 v[68:69], v3 offset:14336\n"
      "ds_read_b64_tr_b16 v[70:71], v3 offset:15360\n"
      "s_waitcnt lgkmcnt(4)\n"
      "s_waitcnt vmcnt(13)\n"
      "v_mfma_f32_32x32x16_bf16 v[152:167], v[80:83], v[144:147], v[152:167]\n"
      "v_mfma_f32_32x32x16_bf16 v[168:183], v[84:87], v[144:147], v[168:183]\n"
      "s_waitcnt vmcnt(0)\n"
      "s_lshl2_add_u32 s70, s99, 160848\n"
      "v_mov_b32 v14, s70\n"
      "v_mov_b32 v15, 1\n"
      "ds_write_b32 v14, v15\n"
      "s_mov_b32 s76, 0x8000\n"
      "s_mov_b64 exec, 15\n"
      ".Lpl_78_%=:\n"
      "v_cmp_ne_u64 vcc, s[58:59], v[10:11]\n"
      "s_cbranch_vccz .Lpd_79_%=\n"
      "s_sub_u32 s76, s76, 1\n"
      "s_cbranch_scc1 .Lpd_79_%=\n"
      "s_sleep 2\n"
      "s_waitcnt vmcnt(0)\n"
      "v_mov_b64 v[10:11], 0\n"
      "buffer_atomic_or_x2 v[10:11], v252, s[16:19], s41 offen sc0 sc1\n"
      "s_waitcnt vmcnt(0)\n"
      "s_branch .Lpl_78_%=\n"
      ".Lpd_79_%=:\n"
      "s_mov_b64 exec, -1\n"
      "s_add_u32 s44, s48, 0\n"
      "buffer_load_dwordx4 v[88:91], v1, s[16:19], s44 offen offset:0 sc1\n"
      "buffer_load_dwordx4 v[92:95], v1, s[16:19], s44 offen offset:1024 sc1\n"
      "buffer_load_dwordx4 v[96:99], v1, s[16:19], s44 offen offset:2048 sc1\n"
      "buffer_load_dwordx4 v[100:103], v1, s[16:19], s44 offen offset:3072 sc1\n"
      "s_add_u32 s44, s48, 4096\n"
      "buffer_load_dwordx4 v[104:107], v1, s[16:19], s44 offen offset:0 sc1\n"
      "buffer_load_dwordx4 v[108:111], v1, s[16:19], s44 offen offset:1024 sc1\n"
      "buffer_load_dwordx4 v[112:115], v1, s[16:19], s44 offen offset:2048 sc1\n"
      "buffer_load_dwordx4 v[116:119], v1, s[16:19], s44 offen offset:3072 sc1\n"
      "s_add_u32 s44, s48, 16384\n"
      "buffer_load_dwordx4 v[120:123], v1, s[16:19], s44 offen offset:0 sc1\n"
      "buffer_load_dwordx4 v[124:127], v1, s[16:19], s44 offen offset:1024 sc1\n"
      "buffer_load_dwordx4 v[128:131], v1, s[16:19], s44 offen offset:2048 sc1\n"
      "buffer_load_dwordx4 v[132:135], v1, s[16:19], s44 offen offset:3072 sc1\n"
      "s_add_u32 s44, s48, 20480\n"
      "buffer_load_dwordx4 v[136:139], v1, s[16:19], s44 offen offset:0 sc1\n"
      "buffer_load_dwordx4 v[140:143], v1, s[16:19], s44 offen offset:1024 sc1\n"
      "s_waitcnt lgkmcnt(0)\n"
      "v_mfma_f32_32x32x16_bf16 v[152:167], v[64:67], v[148:151], v[152:167]\n"
      "buffer_load_dwordx4 v[144:147], v1, s[16:19], s44 offen offset:2048 sc1\n"
      "v_mfma_f32_32x32x16_bf16 v[168:183], v[68:71], v[148:151], v[168:183]\n"
      "buffer_load_dwordx4 v[148:151], v1, s[16:19], s44 offen offset:3072 sc1\n"
      "s_setprio 3\n"
      "v_and_b32 v9, 31, v0\n"
      "v_lshrrev_b32 v8, 5, v0\n"
      "v_and_b32 v15, 7, v0\n"
      "v_xor_b32 v8, v8, v15\n"
      "v_lshlrev_b32 v8, 4, v8\n"
      "s_and_b32 s62, s99, 1\n"
      "s_lshl_b32 s62, s62, 5\n"
      "s_lshr_b32 s63, s99, 1\n"
      "s_mul_i32 s63, s63, 24576\n"
      "v_add_u32 v9, s62, v9\n"
      "v_lshl_or_b32 v8, v9, 8, v8\n"
      "v_add_u32 v8, s63, v8\n"
      "v_cmp_lt_u32 vcc, 47, v9\n"
      "v_add_u32 v15, 98304, v8\n"
      "v_add_u32 v8, -12288, v8\n"
      "v_cndmask_b32 v60, v15, v8, vcc\n"
      "v_xor_b32 v61, 32, v60\n"
      "v_xor_b32 v62, 64, v60\n"
      "v_xor_b32 v63, 96, v60\n"
      "v_mov_b32 v18, 160816\n"
      "s_mov_b32 s76, 0x400000\n"
      ".Llw_80_%=:\n"
      "ds_read_b128 v[248:251], v18\n"
      "s_waitcnt lgkmcnt(0)\n"
      "v_min3_u32 v248, v248, v249, v250\n"
      "v_min_u32 v248, v248, v251\n"
      "v_cmp_le_u32 vcc, 1, v248\n"
      "s_cbranch_vccnz .Llwx_81_%=\n"
      "s_sub_u32 s76, s76, 1\n"
      "s_cbranch_scc0 .Llw_80_%=\n"
      ".Llwx_81_%=:\n"
      "ds_write_b128 v60, v[152:155] offset:0\n"
      "ds_write_b128 v61, v[156:159] offset:0\n"
      "ds_write_b128 v62, v[160:163] offset:0\n"
      "ds_write_b128 v63, v[164:167] offset:0\n"
      "ds_write_b128 v60, v[168:171] offset:128\n"
      "ds_write_b128 v61, v[172:175] offset:128\n"
      "ds_write_b128 v62, v[176:179] offset:128\n"
      "ds_write_b128 v63, v[180:183] offset:128\n"
      "s_lshl2_add_u32 s70, s99, 160816\n"
      "v_mov_b32 v18, s70\n"
      "v_mov_b32 v19, 2\n"
      "ds_write_b32 v18, v19\n"
      "v_lshrrev_b32 v9, 3, v0\n"
      "v_and_b32 v14, 7, v0\n"
      "v_lshlrev_b32 v8, 1, v14\n"
      "v_xor_b32 v8, v8, v9\n"
      "v_lshlrev_b32 v8, 4, v8\n"
      "v_lshl_or_b32 v8, v9, 8, v8\n"
      "s_lshl_b32 s63, s32, 12\n"
      "s_add_u32 s62, s63, 98304\n"
      "s_cmp_eq_u32 s32, 3\n"
      "s_cselect_b32 s62, 0, s62\n"
      "v_add_u32 v16, s62, v8\n"
      "v_xor_b32 v17, 16, v16\n"
      "s_lshr_b32 s62, s32, 1\n"
      "s_mul_i32 s62, s62, 12288\n"
      "s_and_b32 s63, s32, 1\n"
      "s_add_u32 s63, s63, 1\n"
      "s_lshl_b32 s63, s63, 12\n"
      "s_add_u32 s62, s62, s63\n"
      "v_add_u32 v22, s62, v8\n"
      "v_xor_b32 v23, 16, v22\n"
      "v_lshlrev_b32 v14, 4, v14\n"
      "v_lshl_or_b32 v20, v9, 10, v14\n"
      "s_lshl_b32 s63, s32, 6\n"
      "s_add_u32 s63, s63, 161024\n"
      "v_lshl_add_u32 v21, v9, 2, s63\n"
      "s_lshl2_add_u32 s66, s36, s32\n"
      "s_lshl_b32 s66, s66, 14\n"
      "s_lshl_b32 s63, s37, 7\n"
      "s_add_u32 s66, s66, s63\n"
      "s_add_u32 s67, s66, 8192\n"
      "s_lshl2_add_u32 s40, s2, s32\n"
      "s_lshl_b32 s40, s40, 12\n"
      "s_xor_b32 s42, s40, 0x4000\n"
      "s_add_u32 s40, s40, 11534336\n"
      "s_add_u32 s42, s42, 11534336\n"
      "s_add_u32 s64, s54, 524288\n"
      "s_xor_b32 s43, s64, 0x2000\n"
      "s_lshr_b32 s70, s99, 1\n"
      "s_lshl2_add_u32 s70, s70, 160816\n"
      "v_mov_b32 v18, s70\n"
      "s_mov_b32 s76, 0x400000\n"
      ".Llp_82_%=:\n"
      "ds_read2_b32 v[248:249], v18 offset1:2\n"
      "s_waitcnt lgkmcnt(0)\n"
      "v_min_u32 v248, v248, v249\n"
      "v_cmp_le_u32 vcc, 2, v248\n"
      "s_cbranch_vccnz .Llpx_83_%=\n"
      "s_sub_u32 s76, s76, 1\n"
      "s_sleep 1\n"
      "s_cbranch_scc0 .Llp_82_%=\n"
      ".Llpx_83_%=:\n"
      "ds_read_b128 v[24:27], v16 offset:0\n"
      "ds_read_b128 v[28:31], v17 offset:0\n"
      "ds_read_b128 v[32:35], v16 offset:2048\n"
      "ds_read_b128 v[36:39], v17 offset:2048\n"
      "ds_read_b128 v[40:43], v16 offset:24576\n"
      "ds_read_b128 v[44:47], v17 offset:24576\n"
      "ds_read_b128 v[48:51], v16 offset:26624\n"
      "ds_read_b128 v[52:55], v17 offset:26624\n"
      "s_waitcnt lgkmcnt(0)\n"
      "s_lshl2_add_u32 s70, s99, 160816\n"
      "v_mov_b32 v18, s70\n"
      "v_mov_b32 v19, 3\n"
      "ds_write_b32 v18, v19\n"
      "s_setprio 0\n"
      "v_add_f32 v24, v24, v40\n"
      "v_add_f32 v25, v25, v41\n"
      "v_add_f32 v26, v26, v42\n"
      "v_add_f32 v27, v27, v43\n"
      "v_add_f32 v28, v28, v44\n"
      "v_add_f32 v29, v29, v45\n"
      "v_add_f32 v30, v30, v46\n"
      "v_add_f32 v31, v31, v47\n"
      "v_add_f32 v32, v32, v48\n"
      "v_add_f32 v33, v33, v49\n"
      "v_add_f32 v34, v34, v50\n"
      "v_add_f32 v35, v35, v51\n"
      "v_add_f32 v36, v36, v52\n"
      "v_add_f32 v37, v37, v53\n"
      "v_add_f32 v38, v38, v54\n"
      "v_add_f32 v39, v39, v55\n"
      "v_mov_b32 v18, 160816\n"
      "s_mov_b32 s76, 0x400000\n"
      ".Llw_84_%=:\n"
      "ds_read_b128 v[248:251], v18\n"
      "s_waitcnt lgkmcnt(0)\n"
      "v_min3_u32 v248, v248, v249, v250\n"
      "v_min_u32 v248, v248, v251\n"
      "v_cmp_le_u32 vcc, 3, v248\n"
      "s_cbranch_vccnz .Llwx_85_%=\n"
      "s_sub_u32 s76, s76, 1\n"
      "s_cbranch_scc0 .Llw_84_%=\n"
      ".Llwx_85_%=:\n"
      "s_mov_b32 m0, s0\n"
      "s_nop 0\n"
      "buffer_load_dwordx4 v4, s[12:15], s31 offen offset:0 lds\n"
      "buffer_load_dwordx4 v56, s[12:15], s31 offen offset:1024 lds\n"
      "buffer_load_dwordx4 v57, s[12:15], s31 offen offset:2048 lds\n"
      "buffer_load_dwordx4 v58, s[12:15], s31 offen offset:3072 lds\n"
      "s_mov_b32 m0, s1\n"
      "s_add_u32 s31, s31, 0x8000\n"
      "buffer_load_dwordx4 v4, s[12:15], s31 offen offset:0 lds\n"
      "buffer_load_dwordx4 v56, s[12:15], s31 offen offset:1024 lds\n"
      "buffer_load_dwordx4 v57, s[12:15], s31 offen offset:2048 lds\n"
      "buffer_load_dwordx4 v58, s[12:15], s31 offen offset:3072 lds\n"
      "s_waitcnt vmcnt(23)\n"
      "v_mfma_f32_32x32x16_bf16 v[152:167], v[184:187], v[88:91], 0\n"
      "v_mfma_f32_32x32x16_bf16 v[168:183], v[188:191], v[88:91], 0\n"
      "s_waitcnt vmcnt(22)\n"
      "v_mfma_f32_32x32x16_bf16 v[152:167], v[192:195], v[92:95], v[152:167]\n"
      "v_mfma_f32_32x32x16_bf16 v[168:183], v[196:199], v[92:95], v[168:183]\n"
      "s_waitcnt vmcnt(21)\n"
      "v_mfma_f32_32x32x16_bf16 v[152:167], v[200:203], v[96:99], v[152:167]\n"
      "v_mfma_f32_32x32x16_bf16 v[168:183], v[204:207], v[96:99], v[168:183]\n"
      "s_waitcnt vmcnt(20)\n"
      "v_mfma_f32_32x32x16_bf16 v[152:167], v[208:211], v[100:103], v[152:167]\n"
      "v_mfma_f32_32x32x16_bf16 v[168:183], v[212:215], v[100:103], v[168:183]\n"
      "s_waitcnt vmcnt(19)\n"
      "v_mfma_f32_32x32x16_bf16 v[152:167], v[216:219], v[104:107], v[152:167]\n"
      "v_mfma_f32_32x32x16_bf16 v[168:183], v[220:223], v[104:107], v[168:183]\n"
      "s_waitcnt vmcnt(18)\n"
      "v_mfma_f32_32x32x16_bf16 v[152:167], v[224:227], v[108:111], v[152:167]\n"
      "v_mfma_f32_32x32x16_bf16 v[168:183], v[228:231], v[108:111], v[168:183]\n"
      "s_waitcnt vmcnt(17)\n"
      "v_mfma_f32_32x32x16_bf16 v[152:167], v[232:235], v[112:115], v[152:167]\n"
      "ds_read_b64_tr_b16 v[64:65], v5 offset:12288\n"
      "ds_read_b64_tr_b16 v[66:67], v5 offset:13312\n"
      "v_mfma_f32_32x32x16_bf16 v[168:183], v[236:239], v[112:115], v[168:183]\n"
      "ds_read_b64_tr_b16 v[68:69], v6 offset:12288\n"
      "ds_read_b64_tr_b16 v[70:71], v6 offset:13312\n"
      "s_waitcnt vmcnt(16)\n"
      "v_mfma_f32_32x32x16_bf16 v[152:167], v[240:243], v[116:119], v[152:167]\n"
      "ds_read_b64_tr_b16 v[72:73], v5 offset:14336\n"
      "ds_read_b64_tr_b16 v[74:75], v5 offset:15360\n"
      "v_mfma_f32_32x32x16_bf16 v[168:183], v[244:247], v[116:119], v[168:183]\n"
      "ds_read_b64_tr_b16 v[76:77], v6 offset:14336\n"
      "ds_read_b64_tr_b16 v[78:79], v6 offset:15360\n"
      "s_or_b32 s62, s2, s32\n"
      "s_cbranch_scc1 .Lnb_86_%=\n"
      "v_lshrrev_b32 v7, 1, v0\n"
      "v_and_b32 v9, 1, v0\n"
      "v_lshlrev_b32 v9, 8, v9\n"
      "v_lshl_or_b32 v7, v7, 13, v9\n"
      "s_mov_b32 s62, 15728640\n"
      "s_mov_b32 s63, 15728656\n"
      "v_mov_b64 v[8:9], 0\n"
      "buffer_atomic_or_x2 v[8:9], v7, s[16:19], s62 offen sc0 sc1\n"
      "v_mov_b64 v[10:11], 0\n"
      "buffer_atomic_or_x2 v[10:11], v7, s[16:19], s63 offen sc0 sc1\n"
      "s_waitcnt vmcnt(0)\n"
      "s_mov_b32 s76, 0x8000\n"
      ".Lpl_87_%=:\n"
      "v_cmp_ne_u64 vcc, s[58:59], v[8:9]\n"
      "s_cbranch_vccz .Lpd_88_%=\n"
      "s_sub_u32 s76, s76, 1\n"
      "s_cbranch_scc1 .Lpd_88_%=\n"
      "s_sleep 2\n"
      "s_waitcnt vmcnt(0)\n"
      "v_mov_b64 v[8:9], 0\n"
      "buffer_atomic_or_x2 v[8:9], v7, s[16:19], s62 offen sc0 sc1\n"
      "s_waitcnt vmcnt(0)\n"
      "s_branch .Lpl_87_%=\n"
      ".Lpd_88_%=:\n"
      "s_mov_b32 s76, 0x8000\n"
      ".Lpl_89_%=:\n"
      "v_cmp_ne_u64 vcc, s[58:59], v[10:11]\n"
      "s_cbranch_vccz .Lpd_90_%=\n"
      "s_sub_u32 s76, s76, 1\n"
      "s_cbranch_scc1 .Lpd_90_%=\n"
      "s_sleep 2\n"
      "s_waitcnt vmcnt(0)\n"
      "v_mov_b64 v[10:11], 0\n"
      "buffer_atomic_or_x2 v[10:11], v7, s[16:19], s63 offen sc0 sc1\n"
      "s_waitcnt vmcnt(0)\n"
      "s_branch .Lpl_89_%=\n"
      ".Lpd_90_%=:\n"
      ".Lnb_86_%=:\n"
      "s_lshr_b32 s70, s99, 1\n"
      "s_lshl2_add_u32 s70, s70, 160784\n"
      "v_mov_b32 v18, s70\n"
      "s_mov_b32 s76, 0x400000\n"
      ".Llp_91_%=:\n"
      "ds_read2_b32 v[248:249], v18 offset1:2\n"
      "s_waitcnt lgkmcnt(0)\n"
      "v_min_u32 v248, v248, v249\n"
      "v_cmp_le_u32 vcc, 5, v248\n"
      "s_cbranch_vccnz .Llpx_92_%=\n"
      "s_sub_u32 s76, s76, 1\n"
      "s_sleep 1\n"
      "s_cbranch_scc0 .Llp_91_%=\n"
      ".Llpx_92_%=:\n"
      "s_setprio 3\n"
      "ds_read_b128 v[40:43], v22 offset:0\n"
      "ds_read_b128 v[44:47], v23 offset:0\n"
      "ds_read_b128 v[48:51], v22 offset:2048\n"
      "ds_read_b128 v[52:55], v23 offset:2048\n"
      "ds_read_b128 v[88:91], v22 offset:24576\n"
      "ds_read_b128 v[92:95], v23 offset:24576\n"
      "ds_read_b128 v[96:99], v22 offset:26624\n"
      "ds_read_b128 v[100:103], v23 offset:26624\n"
      "s_waitcnt lgkmcnt(4)\n"
      "v_add_f32 v24, v24, v40\n"
      "v_add_f32 v25, v25, v41\n"
      "v_add_f32 v26, v26, v42\n"
      "v_add_f32 v27, v27, v43\n"
      "v_add_f32 v28, v28, v44\n"
      "v_add_f32 v29, v29, v45\n"
      "v_add_f32 v30, v30, v46\n"
      "v_add_f32 v31, v31, v47\n"
      "v_add_f32 v32, v32, v48\n"
      "v_add_f32 v33, v33, v49\n"
      "v_add_f32 v34, v34, v50\n"
      "v_add_f32 v35, v35, v51\n"
      "v_add_f32 v36, v36, v52\n"
      "v_add_f32 v37, v37, v53\n"
      "v_add_f32 v38, v38, v54\n"
      "v_add_f32 v39, v39, v55\n"
      "s_waitcnt vmcnt(0) lgkmcnt(0)\n"
      "v_add_f32 v24, v24, v88\n"
      "v_add_f32 v25, v25, v89\n"
      "v_add_f32 v26, v26, v90\n"
      "v_add_f32 v27, v27, v91\n"
      "buffer_store_dwordx4 v[24:27], v1, s[16:19], s40 offen sc1\n"
      "v_add_f32 v28, v28, v92\n"
      "v_add_f32 v29, v29, v93\n"
      "v_add_f32 v30, v30, v94\n"
      "v_add_f32 v31, v31, v95\n"
      "buffer_store_dwordx4 v[28:31], v1, s[16:19], s40 offen offset:1024 sc1\n"
      "v_add_f32 v32, v32, v96\n"
      "v_add_f32 v33, v33, v97\n"
      "v_add_f32 v34, v34, v98\n"
      "v_add_f32 v35, v35, v99\n"
      "buffer_store_dwordx4 v[32:35], v1, s[16:19], s40 offen offset:2048 sc1\n"
      "v_add_f32 v36, v36, v100\n"
      "v_add_f32 v37, v37, v101\n"
      "v_add_f32 v38, v38, v102\n"
      "v_add_f32 v39, v39, v103\n"
      "buffer_store_dwordx4 v[36:39], v1, s[16:19], s40 offen offset:3072 sc1\n"
      "s_add_u32 s44, s48, 12288\n"
      "buffer_load_dwordx4 v[88:91], v1, s[16:19], s44 offen offset:0 sc1\n"
      "buffer_load_dwordx4 v[92:95], v1, s[16:19], s44 offen offset:1024 sc1\n"
      "buffer_load_dwordx4 v[96:99], v1, s[16:19], s44 offen offset:2048 sc1\n"
      "buffer_load_dwordx4 v[100:103], v1, s[16:19], s44 offen offset:3072 sc1\n"
      "s_lshl2_add_u32 s70, s99, 160816\n"
      "v_mov_b32 v18, s70\n"
      "v_mov_b32 v19, 4\n"
      "ds_write_b32 v18, v19\n"
      "s_setprio 2\n"
      "s_waitcnt lgkmcnt(4)\n"
      "v_mfma_f32_32x32x16_bf16 v[152:167], v[64:67], v[120:123], v[152:167]\n"
      "ds_read_b64_tr_b16 v[80:81], v5 offset:16384\n"
      "ds_read_b64_tr_b16 v[82:83], v5 offset:17408\n"
      "v_mfma_f32_32x32x16_bf16 v[168:183], v[68:71], v[120:123], v[168:183]\n"
      "ds_read_b64_tr_b16 v[84:85], v6 offset:16384\n"
      "ds_read_b64_tr_b16 v[86:87], v6 offset:17408\n"
      "s_waitcnt lgkmcnt(4)\n"
      "v_mfma_f32_32x32x16_bf16 v[152:167], v[72:75], v[124:127], v[152:167]\n"
      "ds_read_b64_tr_b16 v[64:65], v5 offset:18432\n"
      "ds_read_b64_tr_b16 v[66:67], v5 offset:19456\n"
      "v_mfma_f32_32x32x16_bf16 v[168:183], v[76:79], v[124:127], v[168:183]\n"
      "ds_read_b64_tr_b16 v[68:69], v6 offset:18432\n"
      "ds_read_b64_tr_b16 v[70:71], v6 offset:19456\n"
      "s_waitcnt lgkmcnt(4)\n"
      "v_mfma_f32_32x32x16_bf16 v[152:167], v[80:83], v[128:131], v[152:167]\n"
      "ds_read_b64_tr_b16 v[72:73], v5 offset:20480\n"
      "ds_read_b64_tr_b16 v[74:75], v5 offset:21504\n"
      "v_mfma_f32_32x32x16_bf16 v[168:183], v[84:87], v[128:131], v[168:183]\n"
      "ds_read_b64_tr_b16 v[76:77], v6 offset:20480\n"
      "ds_read_b64_tr_b16 v[78:79], v6 offset:21504\n"
      "s_waitcnt lgkmcnt(4)\n"
      "v_mfma_f32_32x32x16_bf16 v[152:167], v[64:67], v[132:135], v[152:167]\n"
      "ds_read_b64_tr_b16 v[80:81], v5 offset:22528\n"
      "ds_read_b64_tr_b16 v[82:83], v5 offset:23552\n"
      "v_mfma_f32_32x32x16_bf16 v[168:183], v[68:71], v[132:135], v[168:183]\n"
      "ds_read_b64_tr_b16 v[84:85], v6 offset:22528\n"
      "ds_read_b64_tr_b16 v[86:87], v6 offset:23552\n"
      "s_waitcnt lgkmcnt(4)\n"
      "v_mfma_f32_32x32x16_bf16 v[152:167], v[72:75], v[136:139], v[152:167]\n"
      "ds_read_b64_tr_b16 v[64:65], v2 offset:12288\n"
      "ds_read_b64_tr_b16 v[66:67], v2 offset:13312\n"
      "v_mfma_f32_32x32x16_bf16 v[168:183], v[76:79], v[136:139], v[168:183]\n"
      "ds_read_b64_tr_b16 v[68:69], v3 offset:12288\n"
      "ds_read_b64_tr_b16 v[70:71], v3 offset:13312\n"
      "s_waitcnt lgkmcnt(4)\n"
      "v_mfma_f32_32x32x16_bf16 v[152:167], v[80:83], v[140:143], v[152:167]\n"
      "ds_read_b64_tr_b16 v[72:73], v2 offset:14336\n"
      "ds_read_b64_tr_b16 v[74:75], v2 offset:15360\n"
      "v_mfma_f32_32x32x16_bf16 v[168:183], v[84:87], v[140:143], v[168:183]\n"
      "ds_read_b64_tr_b16 v[76:77], v3 offset:14336\n"
      "ds_read_b64_tr_b16 v[78:79], v3 offset:15360\n"
      "s_waitcnt lgkmcnt(4)\n"
      "v_mfma_f32_32x32x16_bf16 v[152:167], v[64:67], v[144:147], v[152:167]\n"
      "ds_read_b64_tr_b16 v[80:81], v2 offset:49152\n"
      "ds_read_b64_tr_b16 v[82:83], v2 offset:50176\n"
      "v_mfma_f32_32x32x16_bf16 v[168:183], v[68:71], v[144:147], v[168:183]\n"
      "ds_read_b64_tr_b16 v[84:85], v3 offset:49152\n"
      "ds_read_b64_tr_b16 v[86:87], v3 offset:50176\n"
      "s_waitcnt lgkmcnt(4)\n"
      "s_lshl2_add_u32 s70, s99, 160816\n"
      "v_mov_b32 v18, s70\n"
      "v_mov_b32 v19, 5\n"
      "ds_write_b32 v18, v19\n"
      "v_mfma_f32_32x32x16_bf16 v[152:167], v[72:75], v[148:151], v[152:167]\n"
      "ds_read_b64_tr_b16 v[64:65], v2 offset:51200\n"
      "ds_read_b64_tr_b16 v[66:67], v2 offset:52224\n"
      "v_mfma_f32_32x32x16_bf16 v[168:183], v[76:79], v[148:151], v[168:183]\n"
      "ds_read_b64_tr_b16 v[68:69], v3 offset:51200\n"
      "ds_read_b64_tr_b16 v[70:71], v3 offset:52224\n"
      "s_waitcnt vmcnt(4)\n"
      "s_mov_b64 exec, 1\n"
      "buffer_atomic_swap_x2 v[12:13], v252, s[16:19], s43 offen sc1\n"
      "s_mov_b64 exec, -1\n"
      "s_waitcnt lgkmcnt(4)\n"
      "s_waitcnt vmcnt(4)\n"
      "v_mfma_f32_32x32x16_bf16 v[152:167], v[80:83], v[88:91], v[152:167]\n"
      "ds_read_b64_tr_b16 v[72:73], v2 offset:61440\n"
      "ds_read_b64_tr_b16 v[74:75], v2 offset:62464\n"
      "v_mfma_f32_32x32x16_bf16 v[168:183], v[84:87], v[88:91], v[168:183]\n"
      "ds_read_b64_tr_b16 v[76:77], v3 offset:61440\n"
      "ds_read_b64_tr_b16 v[78:79], v3 offset:62464\n"
      "s_waitcnt lgkmcnt(4)\n"
      "s_waitcnt vmcnt(3)\n"
      "v_mfma_f32_32x32x16_bf16 v[152:167], v[64:67], v[92:95], v[152:167]\n"
      "ds_read_b64_tr_b16 v[80:81], v2 offset:63488\n"
      "ds_read_b64_tr_b16 v[82:83], v2 offset:64512\n"
      "v_mfma_f32_32x32x16_bf16 v[168:183], v[68:71], v[92:95], v[168:183]\n"
      "ds_read_b64_tr_b16 v[84:85], v3 offset:63488\n"
      "ds_read_b64_tr_b16 v[86:87], v3 offset:64512\n"
      "s_mov_b64 exec, 1\n"
      "v_mov_b64 v[8:9], 0\n"
      "buffer_atomic_or_x2 v[8:9], v252, s[16:19], s64 offen sc0 sc1\n"
      "s_mov_b64 exec, -1\n"
      "s_waitcnt lgkmcnt(4)\n"
      "s_waitcnt vmcnt(3)\n"
      "v_mfma_f32_32x32x16_bf16 v[152:167], v[72:75], v[96:99], v[152:167]\n"
      "v_mfma_f32_32x32x16_bf16 v[168:183], v[76:79], v[96:99], v[168:183]\n"
      "s_waitcnt lgkmcnt(0)\n"
      "s_waitcnt vmcnt(2)\n"
      "v_mfma_f32_32x32x16_bf16 v[152:167], v[80:83], v[100:103], v[152:167]\n"
      "v_mfma_f32_32x32x16_bf16 v[168:183], v[84:87], v[100:103], v[168:183]\n"
      "v_mov_b32 v18, 160816\n"
      "s_mov_b32 s76, 0x400000\n"
      ".Llw_93_%=:\n"
      "ds_read_b128 v[248:251], v18\n"
      "s_waitcnt lgkmcnt(0)\n"
      "v_min3_u32 v248, v248, v249, v250\n"
      "v_min_u32 v248, v248, v251\n"
      "v_cmp_le_u32 vcc, 5, v248\n"
      "s_cbranch_vccnz .Llwx_94_%=\n"
      "s_sub_u32 s76, s76, 1\n"
      "s_cbranch_scc0 .Llw_93_%=\n"
      ".Llwx_94_%=:\n"
      "s_nop 0\n"
      "ds_write_b128 v60, v[152:155] offset:12288\n"
      "ds_write_b128 v61, v[156:159] offset:12288\n"
      "ds_write_b128 v62, v[160:163] offset:12288\n"
      "ds_write_b128 v63, v[164:167] offset:12288\n"
      "ds_write_b128 v60, v[168:171] offset:12416\n"
      "ds_write_b128 v61, v[172:175] offset:12416\n"
      "ds_write_b128 v62, v[176:179] offset:12416\n"
      "ds_write_b128 v63, v[180:183] offset:12416\n"
      "s_lshl2_add_u32 s70, s99, 160816\n"
      "v_mov_b32 v18, s70\n"
      "v_mov_b32 v19, 6\n"
      "ds_write_b32 v18, v19\n"
      "s_waitcnt vmcnt(0)\n"
      "s_mov_b32 s76, 0x8000\n"
      "s_mov_b64 exec, 1\n"
      ".Lpl_95_%=:\n"
      "v_cmp_ne_u64 vcc, s[58:59], v[8:9]\n"
      "s_cbranch_vccz .Lpd_96_%=\n"
      "s_sub_u32 s76, s76, 1\n"
      "s_cbranch_scc1 .Lpd_96_%=\n"
      "s_sleep 2\n"
      "s_waitcnt vmcnt(0)\n"
      "v_mov_b64 v[8:9], 0\n"
      "buffer_atomic_or_x2 v[8:9], v252, s[16:19], s64 offen sc0 sc1\n"
      "s_waitcnt vmcnt(0)\n"
      "s_branch .Lpl_95_%=\n"
      ".Lpd_96_%=:\n"
      "s_mov_b64 exec, -1\n"
      "buffer_load_dwordx4 v[24:27], v1, s[16:19], s42 offen offset:0 sc1\n"
      "buffer_load_dwordx4 v[28:31], v1, s[16:19], s42 offen offset:1024 sc1\n"
      "buffer_load_dwordx4 v[32:35], v1, s[16:19], s42 offen offset:2048 sc1\n"
      "buffer_load_dwordx4 v[36:39], v1, s[16:19], s42 offen offset:3072 sc1\n"
      "s_lshr_b32 s70, s99, 1\n"
      "s_lshl2_add_u32 s70, s70, 160816\n"
      "v_mov_b32 v18, s70\n"
      "s_mov_b32 s76, 0x400000\n"
      ".Llp_97_%=:\n"
      "ds_read2_b32 v[248:249], v18 offset1:2\n"
      "s_waitcnt lgkmcnt(0)\n"
      "v_min_u32 v248, v248, v249\n"
      "v_cmp_le_u32 vcc, 6, v248\n"
      "s_cbranch_vccnz .Llpx_98_%=\n"
      "s_sub_u32 s76, s76, 1\n"
      "s_sleep 1\n"
      "s_cbranch_scc0 .Llp_97_%=\n"
      ".Llpx_98_%=:\n"
      "ds_read_b128 v[40:43], v16 offset:12288\n"
      "ds_read_b128 v[44:47], v17 offset:12288\n"
      "ds_read_b128 v[48:51], v16 offset:14336\n"
      "ds_read_b128 v[52:55], v17 offset:14336\n"
      "ds_read_b128 v[104:107], v16 offset:36864\n"
      "ds_read_b128 v[108:111], v17 offset:36864\n"
      "ds_read_b128 v[112:115], v16 offset:38912\n"
      "ds_read_b128 v[116:119], v17 offset:38912\n"
      "s_waitcnt lgkmcnt(0)\n"
      "v_add_f32 v40, v40, v104\n"
      "v_add_f32 v41, v41, v105\n"
      "v_add_f32 v42, v42, v106\n"
      "v_add_f32 v43, v43, v107\n"
      "v_add_f32 v44, v44, v108\n"
      "v_add_f32 v45, v45, v109\n"
      "v_add_f32 v46, v46, v110\n"
      "v_add_f32 v47, v47, v111\n"
      "v_add_f32 v48, v48, v112\n"
      "v_add_f32 v49, v49, v113\n"
      "v_add_f32 v50, v50, v114\n"
      "v_add_f32 v51, v51, v115\n"
      "v_add_f32 v52, v52, v116\n"
      "v_add_f32 v53, v53, v117\n"
      "v_add_f32 v54, v54, v118\n"
      "v_add_f32 v55, v55, v119\n"
      "s_barrier\n"
      "ds_read_b32 v14, v21\n"
      "ds_read_b32 v12, v21 offset:32\n"
      "ds_read_b128 v[104:107], v22 offset:0\n"
      "ds_read_b128 v[108:111], v23 offset:0\n"
      "ds_read_b128 v[112:115], v22 offset:2048\n"
      "ds_read_b128 v[116:119], v23 offset:2048\n"
      "ds_read_b128 v[120:123], v22 offset:24576\n"
      "ds_read_b128 v[124:127], v23 offset:24576\n"
      "ds_read_b128 v[128:131], v22 offset:26624\n"
      "ds_read_b128 v[132:135], v23 offset:26624\n"
      "s_waitcnt lgkmcnt(4)\n"
      "v_pk_add_f32 v[40:41], v[40:41], v[104:105]\n"
      "v_pk_add_f32 v[42:43], v[42:43], v[106:107]\n"
      "v_pk_add_f32 v[44:45], v[44:45], v[108:109]\n"
      "v_pk_add_f32 v[46:47], v[46:47], v[110:111]\n"
      "v_pk_add_f32 v[48:49], v[48:49], v[112:113]\n"
      "v_pk_add_f32 v[50:51], v[50:51], v[114:115]\n"
      "v_pk_add_f32 v[52:53], v[52:53], v[116:117]\n"
      "v_pk_add_f32 v[54:55], v[54:55], v[118:119]\n"
      "s_waitcnt lgkmcnt(0)\n"
      "v_pk_add_f32 v[40:41], v[40:41], v[120:121]\n"
      "v_pk_add_f32 v[42:43], v[42:43], v[122:123]\n"
      "v_pk_add_f32 v[44:45], v[44:45], v[124:125]\n"
      "v_pk_add_f32 v[46:47], v[46:47], v[126:127]\n"
      "v_pk_add_f32 v[48:49], v[48:49], v[128:129]\n"
      "v_pk_add_f32 v[50:51], v[50:51], v[130:131]\n"
      "v_pk_add_f32 v[52:53], v[52:53], v[132:133]\n"
      "v_pk_add_f32 v[54:55], v[54:55], v[134:135]\n"
      "s_waitcnt vmcnt(0)\n"
      "v_pk_add_f32 v[40:41], v[40:41], v[24:25]\n"
      "v_pk_add_f32 v[42:43], v[42:43], v[26:27]\n"
      "v_pk_add_f32 v[44:45], v[44:45], v[28:29]\n"
      "v_pk_add_f32 v[46:47], v[46:47], v[30:31]\n"
      "v_pk_mul_f32 v[40:41], v[40:41], v[14:15] op_sel_hi:[1,0]\n"
      "v_pk_mul_f32 v[42:43], v[42:43], v[14:15] op_sel_hi:[1,0]\n"
      "v_pk_mul_f32 v[44:45], v[44:45], v[14:15] op_sel_hi:[1,0]\n"
      "v_pk_mul_f32 v[46:47], v[46:47], v[14:15] op_sel_hi:[1,0]\n"
      "v_cvt_pk_bf16_f32 v96, v40, v41\n"
      "v_cvt_pk_bf16_f32 v97, v42, v43\n"
      "v_cvt_pk_bf16_f32 v98, v44, v45\n"
      "v_cvt_pk_bf16_f32 v99, v46, v47\n"
      "buffer_store_dwordx4 v[96:99], v20, s[20:23], s66 offen\n"
      "v_pk_add_f32 v[48:49], v[48:49], v[32:33]\n"
      "v_pk_add_f32 v[50:51], v[50:51], v[34:35]\n"
      "v_pk_add_f32 v[52:53], v[52:53], v[36:37]\n"
      "v_pk_add_f32 v[54:55], v[54:55], v[38:39]\n"
      "v_pk_mul_f32 v[48:49], v[48:49], v[12:13] op_sel_hi:[1,0]\n"
      "v_pk_mul_f32 v[50:51], v[50:51], v[12:13] op_sel_hi:[1,0]\n"
      "v_pk_mul_f32 v[52:53], v[52:53], v[12:13] op_sel_hi:[1,0]\n"
      "v_pk_mul_f32 v[54:55], v[54:55], v[12:13] op_sel_hi:[1,0]\n"
      "v_cvt_pk_bf16_f32 v100, v48, v49\n"
      "v_cvt_pk_bf16_f32 v101, v50, v51\n"
      "v_cvt_pk_bf16_f32 v102, v52, v53\n"
      "v_cvt_pk_bf16_f32 v103, v54, v55\n"
      "buffer_store_dwordx4 v[100:103], v20, s[20:23], s67 offen\n"
      "s_or_b32 s62, s2, s32\n"
      "s_cbranch_scc1 .Lend_57_%=\n"
      "v_mov_b32 v9, s59\n"
      "s_mov_b64 exec, 1\n"
      "buffer_store_dword v9, off, s[16:19], 0 sc1\n"
      "s_mov_b64 exec, -1\n"
      ".Lend_57_%=:\n"
      ".Lcodeend_1_%=:\n"
      "s_endpgm\n"
      :
      : "{s[0:1]}"(__builtin_amdgcn_kernarg_segment_ptr()), "{s2}"(blockIdx.x), "{v0}"(threadIdx.x)
      : "memory", "vcc", "v255", "s99");
}
extern "C" void kernel_launch(void* const* d_in, const int* in_sizes, int n_in, void* d_out, int out_size, void* d_ws, size_t ws_size,
                              hipStream_t stream) {
  const u32 F = 0x00020000u;
  Params p;
  p.x = {d_in[0], 2048u * 1024u, F};
  p.w2 = {d_in[6], 1792u * 1024u, F};
  p.wo = {d_in[2], 512u * 1024u, F};
  p.ws = {d_ws, 241u << 20, F};
  p.out = {d_out, 2048u * 1024u, F};
  p.w1 = {d_in[4], 512u * 3584u, F};
  p.b1 = {d_in[5], 1792u * 2u, F};
  p.wqkv = {d_in[1], 512u * 3072u, F};
  p.gw = {d_in[7], 1024u, F};
  (void)hipFuncSetAttribute((const void*)fused, hipFuncAttributeMaxDynamicSharedMemorySize, LDS_BYTES);
  fused<<<256, 512, LDS_BYTES, stream>>>(p);
}
```
